```python
import torch

def _softmax(x, axis=-1):
    return torch.softmax(x, dim=axis)
def _gelu(x):
    return torch.nn.functional.gelu(x, approximate="tanh")
def _silu(x):
    return torch.nn.functional.silu(x)
def _sigmoid(x):
    return torch.sigmoid(x)
def _layernorm(x, axis=-1, eps=1e-5):
    m = x.mean(dim=axis, keepdim=True)
    v = x.var(dim=axis, keepdim=True, unbiased=False)
    return (x - m) / torch.sqrt(v + eps)
def _scatter_add(tgt, idx, src, axis=0):
    return tgt.clone().index_add_(axis, idx, src)
def _segment_sum(src, seg, n):
    out = torch.zeros((n,) + src.shape[1:], dtype=src.dtype)
    return out.index_add_(0, seg, src)
def _pad(x, pw):
    flat = tuple(v for pair in reversed(pw) for v in pair)
    return torch.nn.functional.pad(x, flat)

kernel_name = 'recurrent_scan__ssm_conv_s5__glu_gated_glu__prompt'

def setup_inputs():

    _w = (lambda a: (lambda t: t.to(torch.bfloat16) if t.is_floating_point() else t)(torch.as_tensor(a)))
    def _A_setup():
        B, S, D = 1, 2048, 512
        P = D
        Lambda_re = -0.5 * (lambda _x: torch.exp(torch.as_tensor(_x)))((lambda *s: torch.randn(*s, dtype=torch.bfloat16) * 0.2)(P))
        Lambda_im = (lambda *s: torch.randn(*s, dtype=torch.bfloat16) * 0.4)(P) * 1.0
        B_re = (lambda *s: torch.randn(*s, dtype=torch.bfloat16) * 0.2)(P, D) * (D ** -0.5)
        B_im = (lambda *s: torch.randn(*s, dtype=torch.bfloat16) * 0.2)(P, D) * (D ** -0.5)
        C_re = (lambda *s: torch.randn(*s, dtype=torch.bfloat16) * 0.2)(D, P) * (P ** -0.5)
        C_im = (lambda *s: torch.randn(*s, dtype=torch.bfloat16) * 0.2)(D, P) * (P ** -0.5)
        log_dt = (lambda *s: torch.randn(*s, dtype=torch.bfloat16) * 0.2)(P) * 0.3 - 2.0
        Dskip = (lambda *s: torch.randn(*s, dtype=torch.bfloat16) * 0.05)(D)
        return {
            "x": _w((lambda *s: torch.randn(*s, dtype=torch.bfloat16) * 0.05)(B, S, D)),
            "Lambda_re": _w(Lambda_re),
            "Lambda_im": _w(Lambda_im),
            "B_re": _w(B_re),
            "B_im": _w(B_im),
            "C_re": _w(C_re),
            "C_im": _w(C_im),
            "log_dt": _w(log_dt),
            "Dskip": _w(Dskip),
        }
    _A_in = _A_setup()

    def _B_setup():
        B, S, D, F = 1, 2048, 512, 1792
        return {
            "x": _w((lambda *s: torch.randn(*s, dtype=torch.bfloat16) * 0.05)(B, S, D)),
            "w_gate": _w((lambda *s: torch.randn(*s, dtype=torch.bfloat16) * 0.05)(D, F)),
            "w_up": _w((lambda *s: torch.randn(*s, dtype=torch.bfloat16) * 0.05)(D, F)),
            "w_down": _w((lambda *s: torch.randn(*s, dtype=torch.bfloat16) * 0.05)(F, D)),
        }
    _B_in = _B_setup()

    return {
        "A_x": _A_in["x"],
        "A_Lambda_re": _A_in["Lambda_re"],
        "A_Lambda_im": _A_in["Lambda_im"],
        "A_B_re": _A_in["B_re"],
        "A_B_im": _A_in["B_im"],
        "A_C_re": _A_in["C_re"],
        "A_C_im": _A_in["C_im"],
        "A_log_dt": _A_in["log_dt"],
        "A_Dskip": _A_in["Dskip"],
        "B_x": _B_in["x"],
        "B_w_gate": _B_in["w_gate"],
        "B_w_up": _B_in["w_up"],
        "B_w_down": _B_in["w_down"],
    }

def reference(A_x, A_Lambda_re, A_Lambda_im, A_B_re, A_B_im, A_C_re, A_C_im, A_log_dt, A_Dskip, B_x, B_w_gate, B_w_up, B_w_down):
    def _A_ref(x, Lambda_re, Lambda_im, B_re, B_im, C_re, C_im, log_dt, Dskip):
        Bsz, L, H = x.shape
        Lam = Lambda_re + 1j * Lambda_im
        Bmat = B_re + 1j * B_im
        Cmat = C_re + 1j * C_im
        dt = torch.exp(log_dt)
        Lam_bar = torch.exp(dt * Lam)
        B_bar = ((Lam_bar - 1.0) / Lam)[:, None] * Bmat
        Bu = torch.einsum("bld,pd->blp", x + 0j, B_bar)
        h = torch.zeros_like(Bu[:, 0, :])
        hs = []
        for t in range(L):
            h = Lam_bar[None, :] * h + Bu[:, t, :]
            hs.append(h)
        hseq = torch.stack(hs, 1)
        y = 2.0 * torch.einsum("blp,dp->bld", hseq, Cmat).real + x * Dskip[None, None, :]
        return y

    def _B_ref(x, w_gate, w_up, w_down):
        g = torch.einsum("bsd,df->bsf", x, w_gate)
        u = torch.einsum("bsd,df->bsf", x, w_up)
        h = _sigmoid(g) * u
        return torch.einsum("bsf,fd->bsd", h, w_down)

    x = A_x
    xn = _layernorm(x, axis=-1)
    s = _A_ref(xn, A_Lambda_re, A_Lambda_im, A_B_re, A_B_im, A_C_re, A_C_im, A_log_dt, A_Dskip)
    g = _silu(_B_ref(xn, B_w_gate, B_w_up, B_w_down))
    return x + s * g
```

```cpp
#include <hip/hip_runtime.h>
#include <hip/hip_bf16.h>
typedef __hip_bfloat16 bf;
#define L 2048
#define D 512
#define P 512
#define F 1792
__device__ inline float b2f(bf v){ return __bfloat162float(v); }
__global__ void k_ln(const bf* x, float* xn){
  int t = blockIdx.x; __shared__ float red[64];
  float v[8]; float s=0;
  for(int i=0;i<8;i++){ v[i]=b2f(x[t*D+threadIdx.x*8+i]); s+=v[i]; }
  for(int o=32;o;o>>=1) s+=__shfl_xor(s,o);
  float m=s/D; float q=0;
  for(int i=0;i<8;i++){ float d=v[i]-m; q+=d*d; }
  for(int o=32;o;o>>=1) q+=__shfl_xor(q,o);
  float r=rsqrtf(q/D+1e-5f);
  for(int i=0;i<8;i++) xn[t*D+threadIdx.x*8+i]=(v[i]-m)*r;
}
__global__ void k_bu(const float* xn, const bf* Bre, const bf* Bim, float* bu){
  int t=blockIdx.x; int p=threadIdx.x; __shared__ float xs[D];
  xs[p]=xn[t*D+p]; __syncthreads();
  float a=0,b=0;
  for(int k=0;k<D;k++){ a+=xs[k]*b2f(Bre[p*D+k]); b+=xs[k]*b2f(Bim[p*D+k]); }
  bu[(t*P+p)*2]=a; bu[(t*P+p)*2+1]=b;
}
__global__ void k_scan(float* bu, const bf* Lre, const bf* Lim, const bf* ldt){
  int p=blockIdx.x*64+threadIdx.x;
  float lr=b2f(Lre[p]), li=b2f(Lim[p]); float dt=b2f(__float2bfloat16(expf(b2f(ldt[p]))));
  float e=expf(dt*lr); float ar=e*cosf(dt*li), ai=e*sinf(dt*li);
  float nr=ar-1.f, ni=ai; float den=lr*lr+li*li;
  float cr=(nr*lr+ni*li)/den, ci=(ni*lr-nr*li)/den;
  float hr=0,hi=0;
  for(int t=0;t<L;t++){
    float a=bu[(t*P+p)*2], b=bu[(t*P+p)*2+1];
    float ur=cr*a-ci*b, ui=cr*b+ci*a;
    float nhr=ar*hr-ai*hi+ur, nhi=ar*hi+ai*hr+ui; hr=nhr; hi=nhi;
    bu[(t*P+p)*2]=hr; bu[(t*P+p)*2+1]=hi;
  }
}
__global__ void k_glu(const float* xn, const bf* wg, const bf* wu, float* hg){
  int t=blockIdx.x; __shared__ float xs[D];
  for(int i=threadIdx.x;i<D;i+=256) xs[i]=xn[t*D+i]; __syncthreads();
  for(int f=threadIdx.x;f<F;f+=256){
    float g=0,u=0;
    for(int k=0;k<D;k++){ g+=xs[k]*b2f(wg[k*F+f]); u+=xs[k]*b2f(wu[k*F+f]); }
    hg[t*F+f]=u/(1.f+expf(-g));
  }
}
__global__ void k_out(const bf* x, const float* xn, const float* h, const float* hg, const bf* Cre, const bf* Cim, const bf* Dk, const bf* wd, float* out){
  int t=blockIdx.x; int d=threadIdx.x; __shared__ float hs[2*P]; __shared__ float gs[F];
  for(int i=d;i<2*P;i+=512) hs[i]=h[t*2*P+i];
  for(int i=d;i<F;i+=512) gs[i]=hg[t*F+i];
  __syncthreads();
  float y=0; for(int p=0;p<P;p++) y+=hs[2*p]*b2f(Cre[d*P+p])-hs[2*p+1]*b2f(Cim[d*P+p]);
  float dn=0; for(int f=0;f<F;f++) dn+=gs[f]*b2f(wd[f*D+d]);
  float s=2.f*y+xn[t*D+d]*b2f(Dk[d]);
  float g=dn/(1.f+expf(-dn));
  out[t*D+d]=b2f(x[t*D+d])+s*g;
}
extern "C" void kernel_launch(void* const* d_in, const int* in_sizes, int n_in, void* d_out, int out_size, void* d_ws, size_t ws_size, hipStream_t stream){
  const bf* x=(const bf*)d_in[0]; const bf* Lre=(const bf*)d_in[1]; const bf* Lim=(const bf*)d_in[2];
  const bf* Bre=(const bf*)d_in[3]; const bf* Bim=(const bf*)d_in[4]; const bf* Cre=(const bf*)d_in[5]; const bf* Cim=(const bf*)d_in[6];
  const bf* ldt=(const bf*)d_in[7]; const bf* Dk=(const bf*)d_in[8]; const bf* wg=(const bf*)d_in[10]; const bf* wu=(const bf*)d_in[11]; const bf* wd=(const bf*)d_in[12];
  float* xn=(float*)d_ws; float* bu=xn+L*D; float* hg=bu+L*P*2;
  k_ln<<<L,64,0,stream>>>(x,xn);
  k_bu<<<L,512,0,stream>>>(xn,Bre,Bim,bu);
  k_scan<<<P/64,64,0,stream>>>(bu,Lre,Lim,ldt);
  k_glu<<<L,256,0,stream>>>(xn,wg,wu,hg);
  k_out<<<L,512,0,stream>>>(x,xn,bu,hg,Cre,Cim,Dk,wd,(float*)d_out);
}
```

```cpp
#include <hip/hip_runtime.h>
#include <stdint.h>

typedef __attribute__((ext_vector_type(8))) __bf16 bf16x8;
typedef __attribute__((ext_vector_type(2))) __bf16 bf16x2;
typedef __attribute__((ext_vector_type(4))) short s16x4;
typedef __attribute__((ext_vector_type(16))) float f32x16;
typedef __attribute__((ext_vector_type(4))) float f32x4;
typedef __attribute__((ext_vector_type(2))) float f32x2;
typedef __attribute__((ext_vector_type(4))) unsigned u32x4;
typedef __attribute__((ext_vector_type(2))) unsigned u32x2;
#define AS3 __attribute__((address_space(3)))
#define DEV __device__ __forceinline__

#define XSL 16384
#define WR 49152
#define WSL 24576
#define SCR 122880
#define MISC 147456
#define M_R (MISC + 0)
#define M_TF (MISC + 512)
#define M_CF (MISC + 2560)
#define M_PART (MISC + 2560)
#define M_CS (MISC + 6656)
#define M_AGG (MISC + 8192)
#define M_PAR (MISC + 10240)
#define M_PW (MISC + 11776)
#define M_CNT (MISC + 15872)
#define W_EPOCH 0
#define W_ARR 2048
#define W_FLG 65536
#define W_STAT 131072
#define W_CSB 196608
#define W_CSW 262144
#define W_AGG 524288
#define W_HG (1u << 20)
#define W_H (9u << 20)

#define SPIN_MAX (1 << 16)

DEV u32x4 lds_r128(unsigned a) { return *(AS3 u32x4*)(uintptr_t)a; }
DEV f32x4 lds_rf4(unsigned a) { return *(AS3 f32x4*)(uintptr_t)a; }
DEV f32x2 lds_rf2(unsigned a) { return *(AS3 f32x2*)(uintptr_t)a; }
DEV float lds_rf(unsigned a) { return *(AS3 float*)(uintptr_t)a; }
DEV void lds_w128(unsigned a, u32x4 v) { *(AS3 u32x4*)(uintptr_t)a = v; }
DEV void lds_wf4(unsigned a, f32x4 v) { *(AS3 f32x4*)(uintptr_t)a = v; }
DEV void lds_wf2(unsigned a, f32x2 v) { *(AS3 f32x2*)(uintptr_t)a = v; }
DEV void lds_wf(unsigned a, float v) { *(AS3 float*)(uintptr_t)a = v; }
DEV void lds_wu(unsigned a, unsigned v) { *(AS3 unsigned*)(uintptr_t)a = v; }
DEV u32x4 lds_tr2(unsigned a0, unsigned a1) {
  s16x4 lo = __builtin_amdgcn_ds_read_tr16_b64_v4i16((AS3 s16x4*)(uintptr_t)a0);
  s16x4 hi = __builtin_amdgcn_ds_read_tr16_b64_v4i16((AS3 s16x4*)(uintptr_t)a1);
  u32x2 l2 = __builtin_bit_cast(u32x2, lo), h2 = __builtin_bit_cast(u32x2, hi);
  u32x4 r = {l2[0], l2[1], h2[0], h2[1]};
  return r;
}
DEV f32x16 mfma(u32x4 a, u32x4 b, f32x16 c) {
  return __builtin_amdgcn_mfma_f32_32x32x16_bf16(__builtin_bit_cast(bf16x8, a), __builtin_bit_cast(bf16x8, b), c, 0, 0, 0);
}
template <int O> DEV void bl(unsigned voff, u32x4 srd, unsigned soff) {
  asm volatile("buffer_load_dwordx4 %0, %1, %2 offen offset:%3 lds" ::"v"(voff), "s"(srd), "s"(soff), "n"(O));
}
template <int K> DEV void m0n(unsigned b) { asm volatile("s_add_u32 m0, %0, %1\n\ts_nop 0" ::"s"(b), "n"(K) : "scc"); }
template <int K, int D> DEV void m0k(unsigned b, unsigned& k1, unsigned& k2) {
  asm volatile("s_add_u32 m0, %2, %3\n\ts_add_u32 %0, %0, %4\n\ts_and_b32 %0, %0, 0x3ff\n\ts_mul_i32 %1, %0, 0x700"
               : "+s"(k1), "=s"(k2) : "s"(b), "n"(K), "n"(D) : "scc");
}
template <int K, int D> DEV void m0a(unsigned b, unsigned& o, unsigned a) {
  asm volatile("s_add_u32 m0, %1, %2\n\ts_add_u32 %0, %3, %4" : "=s"(o) : "s"(b), "n"(K), "s"(a), "n"(D) : "scc");
}
template <int D> DEV void sadd(unsigned& o, unsigned a) { asm volatile("s_add_u32 %0, %1, %2" : "=s"(o) : "s"(a), "n"(D) : "scc"); }
DEV u32x4 mksrd(const char* p, unsigned bytes) {
  unsigned long long a = (unsigned long long)p - 4096ull;
  u32x4 r = {(unsigned)a, (unsigned)(a >> 32), bytes + 4096u, 0x20000u};
  return r;
}
DEV float dot2(unsigned a, unsigned b, float c) {
  return __builtin_amdgcn_fdot2_f32_bf16(__builtin_bit_cast(bf16x2, a), __builtin_bit_cast(bf16x2, b), c, false);
}
DEV float sw32(float a, float b) {
  u32x2 r = __builtin_amdgcn_permlane32_swap(__float_as_uint(a), __float_as_uint(b), false, false);
  return __uint_as_float(r[0]) + __uint_as_float(r[1]);
}
DEV float sw16(float a, float b) {
  u32x2 r = __builtin_amdgcn_permlane16_swap(__float_as_uint(a), __float_as_uint(b), false, false);
  return __uint_as_float(r[0]) + __uint_as_float(r[1]);
}
DEV void gst16sm(unsigned voff, u32x4 v, void* sbase, unsigned long long m) {
  asm volatile("s_mov_b64 exec, %3\n\tglobal_store_dwordx4 %0, %1, %2 sc1\n\ts_mov_b64 exec, -1\n\ts_nop 0"
               ::"v"(voff), "v"(v), "s"(sbase), "s"(m) : "memory");
}
template <int O> DEV void gld16so(u32x4& r, unsigned voff, const void* sbase) {
  asm volatile("global_load_dwordx4 %0, %1, %2 offset:%3 sc1" : "=v"(r) : "v"(voff), "s"(sbase), "n"(O));
}
DEV void gld16(u32x4& r, unsigned voff, const void* sbase) {
  asm volatile("global_load_dwordx4 %0, %1, %2" : "=v"(r) : "v"(voff), "s"(sbase));
}
DEV void gld16s(u32x4& r, unsigned voff, const void* sbase) {
  asm volatile("global_load_dwordx4 %0, %1, %2 sc1" : "=v"(r) : "v"(voff), "s"(sbase));
}
DEV void gld8(u32x2& r, unsigned voff, const void* sbase) {
  asm volatile("global_load_dwordx2 %0, %1, %2" : "=v"(r) : "v"(voff), "s"(sbase));
}
DEV void gld8s(u32x2& r, unsigned voff, const void* sbase) {
  asm volatile("global_load_dwordx2 %0, %1, %2 sc1" : "=v"(r) : "v"(voff), "s"(sbase));
}
DEV void gld4(unsigned& r, unsigned voff, const void* sbase) {
  asm volatile("global_load_dword %0, %1, %2" : "=v"(r) : "v"(voff), "s"(sbase));
}
DEV void tch4(unsigned& r, unsigned voff, const void* sbase) {
  asm volatile("global_load_dword %0, %1, %2" : "+v"(r) : "v"(voff), "s"(sbase));
}
DEV void gldu16(unsigned& r, unsigned voff, const void* sbase) {
  asm volatile("global_load_ushort %0, %1, %2" : "=v"(r) : "v"(voff), "s"(sbase));
}
DEV void gst16s(unsigned voff, u32x4 v, void* sbase) {
  asm volatile("global_store_dwordx4 %0, %1, %2 sc1\n\ts_nop 1" ::"v"(voff), "v"(v), "s"(sbase) : "memory");
}
DEV void gst8s(unsigned voff, u32x2 v, void* sbase) {
  asm volatile("global_store_dwordx2 %0, %1, %2 sc1\n\ts_nop 1" ::"v"(voff), "v"(v), "s"(sbase) : "memory");
}
DEV void gst16(unsigned voff, f32x4 v, void* sbase) {
  asm volatile("global_store_dwordx4 %0, %1, %2\n\ts_nop 1" ::"v"(voff), "v"(v), "s"(sbase) : "memory");
}
template <int N> DEV void waitv() { asm volatile("s_waitcnt vmcnt(%0)" ::"n"(N) : "memory"); }
template <int N> DEV void barv() { asm volatile("s_waitcnt vmcnt(%0) lgkmcnt(0)\n\ts_barrier" ::"n"(N) : "memory"); }
DEV void bar() { asm volatile("s_waitcnt lgkmcnt(0)\n\ts_barrier" ::: "memory"); }
DEV void waitl() { asm volatile("s_waitcnt lgkmcnt(0)" ::: "memory"); }
#define TIE(x) asm volatile("" : "+v"(x))
#define TIES(x) asm volatile("" : "+s"(x))
#define SCHED() __builtin_amdgcn_sched_barrier(0)
template <int K> DEV unsigned addk(unsigned a) {
  unsigned r;
  asm volatile("v_add_u32 %0, %1, %2" : "=v"(r) : "n"(K), "v"(a));
  return r;
}
DEV float swp(float x) { return __builtin_amdgcn_update_dpp(0.f, x, 0xB1, 0xf, 0xf, true); }
template <int C> DEV float dppadd(float x) { return x + __builtin_amdgcn_update_dpp(0.f, x, C, 0xf, 0xf, true); }
DEV float bflo(unsigned u) { return __uint_as_float(u << 16); }
DEV float bfhi(unsigned u) { return __uint_as_float(u & 0xffff0000u); }
DEV unsigned pk(float a, float b) {
  bf16x2 v = {(__bf16)a, (__bf16)b};
  return __builtin_bit_cast(unsigned, v);
}
DEV float x32sum(float a) {
  u32x2 r = __builtin_amdgcn_permlane32_swap(__float_as_uint(a), __float_as_uint(a), false, false);
  return __uint_as_float(r[0]) + __uint_as_float(r[1]);
}
DEV float x16sum(float a) {
  u32x2 r = __builtin_amdgcn_permlane16_swap(__float_as_uint(a), __float_as_uint(a), false, false);
  return __uint_as_float(r[0]) + __uint_as_float(r[1]);
}
DEV float wave_sum(float v) {
  v = dppadd<0xB1>(v);
  v = dppadd<0x4E>(v);
  v = dppadd<0x141>(v);
  v = dppadd<0x140>(v);
  v = x16sum(v);
  return x32sum(v);
}
DEV int pi32(int r) { return 16 * ((r >> 2) & 1) + 4 * (r >> 3) + (r & 3); }

#define TS(k)
#define TSC(k, a, b)
#define TSARG
#define TSPASS

struct Ctx {
  const char *x, *xb, *wg, *wu, *bre, *bim, *lre, *lim, *ldt, *dsk, *wd, *cre, *cim;
  char *ws, *out;
  int mt, rot, w, wm, lane, g, bid;
  int u0, q, mt64, nt, rE, rL;
  unsigned tag_lo, tag_hi;
};
DEV u32x4 granule(const Ctx& c, float a, float b) {
  unsigned tl = c.tag_lo, th = c.tag_hi;
  TIES(tl); TIES(th);
  u32x4 g = {tl, th, __float_as_uint(a), __float_as_uint(b)};
  return g;
}
DEV void set_flag(const Ctx& c, unsigned off) {
  unsigned tl = c.tag_lo, th = c.tag_hi;
  TIES(tl); TIES(th);
  u32x2 t = {tl, th};
  unsigned z = off;
  asm volatile("s_mov_b64 exec, 1\n\tglobal_store_dwordx2 %0, %1, %2 sc1\n\ts_mov_b64 exec, -1\n\ts_nop 0"
               ::"v"(z), "v"(t), "s"(c.ws) : "memory");
}
DEV bool tag_ok(const Ctx& c, const u32x4& g) { return g[0] == c.tag_lo && g[1] == c.tag_hi; }
#define DPPSW " quad_perm:[1,0,3,2] row_mask:0xf bank_mask:0xf"
DEV float mul1(float a, float b) { float r = a * b; TIE(r); return r; }
DEV float fma1(float a, float b, float c) { float r = __builtin_fmaf(a, b, c); TIE(r); return r; }
DEV unsigned bfb(float a) { return (unsigned)__builtin_bit_cast(unsigned short, (__bf16)a); }
DEV void split3(float x, unsigned& hi, unsigned& mi, unsigned& lo) {
  hi = bfb(x);
  float r = x - __uint_as_float(hi << 16);
  mi = bfb(r);
  lo = bfb(r - __uint_as_float(mi << 16));
}
DEV u32x4 frag_c(float x) {
  unsigned hi, mi, lo;
  split3(x, hi, mi, lo);
  u32x4 r = {hi | (mi << 16), lo | (hi << 16), mi | (hi << 16), 0u};
  return r;
}
DEV u32x4 frag_t(float x) {
  unsigned hi, mi, lo;
  split3(x, hi, mi, lo);
  u32x4 r = {hi | (hi << 16), hi | (mi << 16), mi | (lo << 16), 0u};
  return r;
}
template <int V, int O> DEV void csig() {
  unsigned ad = M_CNT + O, v = V;
  asm volatile("s_mov_b64 exec, 1\n\tds_add_u32 %0, %1\n\ts_mov_b64 exec, -1" ::"v"(ad), "v"(v) : "memory");
}
template <int O> DEV void cwaitx(unsigned v, unsigned mask, unsigned want) {
  int spins = 0;
  while ((__builtin_amdgcn_readfirstlane(v) & mask) != want && ++spins < SPIN_MAX * 64) {
    __builtin_amdgcn_s_sleep(1);
    v = *(volatile AS3 unsigned*)(uintptr_t)(M_CNT + O);
  }
  asm volatile("" ::: "memory");
}
#define cwait cwaitx<8>
struct Glu { float g[8]; u32x4 o; };
template <int S> DEV void glu_st(const f32x16& acc, Glu& q, float nr, float ir) {
  constexpr int B = (S & 1) * 4;
  if ((S >> 1) == 0) {
#pragma unroll
    for (int p = 0; p < 4; p++) q.g[B + p] = mul1(acc[B + p], nr);
#pragma unroll
    for (int p = 0; p < 4; p++) q.g[B + p] = __builtin_amdgcn_exp2f(q.g[B + p]);
  }
  if ((S >> 1) == 1) {
#pragma unroll
    for (int p = 0; p < 4; p++) q.g[B + p] = fma1(q.g[B + p], ir, ir);
#pragma unroll
    for (int p = 0; p < 4; p++) q.g[B + p] = __builtin_amdgcn_rcpf(q.g[B + p]);
  }
  if ((S >> 1) == 2) {
#pragma unroll
    for (int p = 0; p < 4; p++) q.g[B + p] = mul1(acc[8 + B + p], q.g[B + p]);
    q.o[B / 2] = pk(q.g[B], q.g[B + 1]);
    q.o[B / 2 + 1] = pk(q.g[B + 2], q.g[B + 3]);
  }
  SCHED();
}

template <int S> DEV void glu_m(const f32x16& acc, Glu& q, float nr, float ir) {
  if (S >= 1 && S <= 8) { q.g[S - 1] = __builtin_amdgcn_exp2f(q.g[S - 1]); TIE(q.g[S - 1]); }
  if (S >= 9 && S <= 16) { q.g[S - 9] = __builtin_amdgcn_rcpf(q.g[S - 9]); TIE(q.g[S - 9]); }
  if (S < 8) q.g[S] = mul1(acc[S], nr);
  if (S >= 8 && S < 16) q.g[S - 8] = fma1(q.g[S - 8], ir, ir);
  if (S == 18) { q.o[0] = pk(q.g[0], q.g[1]); q.o[1] = pk(q.g[2], q.g[3]); TIE(q.o[0]); TIE(q.o[1]); }
  if (S == 20) { q.o[2] = pk(q.g[4], q.g[5]); q.o[3] = pk(q.g[6], q.g[7]); }
  constexpr int u0 = S == 16 ? 0 : S == 17 ? 1 : S == 18 ? 4 : 5, u1 = S == 16 ? 1 : S == 17 ? 4 : S == 18 ? 5 : S == 19 ? 8 : 0;
#pragma unroll
  for (int p = u0; p < u1; p++) q.g[p] = mul1(acc[8 + p], q.g[p]);
  SCHED();
}

struct Ssm { float u[2][16], k0[2], k2[2]; };
DEV unsigned ssm_e1(const Ctx& c, const f32x16* v, Ssm& S) {
  int lane = c.lane, wm = c.wm;
  TIE(lane);
  int h = lane >> 5, c32 = lane & 31, part = lane & 1, s16 = c32 >> 1;
  unsigned sa = M_R + (32 * wm + 16 * h) * 4;
  float ar[2], si[2], a0 = 0.f, a1 = 0.f;
  f32x4 st[4];
#pragma unroll
  for (int i = 0; i < 4; i++) st[i] = lds_rf4(sa + i * 16);
#pragma unroll
  for (int t = 0; t < 2; t++) {
    f32x4 p = lds_rf4(M_PAR + (t * 16 + s16) * 48);
    ar[t] = p[0];
    si[t] = part ? -p[1] : p[1];
    S.k0[t] = part ? -p[2] : p[2];
    S.k2[t] = p[3];
  }

  asm volatile("s_nop 11");
#pragma unroll
  for (int i = 0; i < 8; i++) {
    float s0, s1, u0, u1;
    asm volatile(
        "v_mul_f32 %0, %8, %12\n\t"
        "v_mul_f32 %1, %10, %12\n\t"
        "v_fma_f32 %4, %14, %18, %0\n\t"
        "v_fma_f32 %5, %15, %19, %1\n\t"
        "v_fmac_f32_dpp %4, %18, %16" DPPSW "\n\t"
        "v_fmac_f32_dpp %5, %19, %17" DPPSW "\n\t"
        "v_mul_f32 %2, %9, %13\n\t"
        "v_mul_f32 %3, %11, %13\n\t"
        "v_fma_f32 %6, %14, %4, %2\n\t"
        "v_fma_f32 %7, %15, %5, %3\n\t"
        "v_fmac_f32_dpp %6, %4, %16" DPPSW "\n\t"
        "v_fmac_f32_dpp %7, %5, %17" DPPSW
        : "=&v"(S.u[0][2 * i]), "=&v"(S.u[1][2 * i]), "=&v"(S.u[0][2 * i + 1]), "=&v"(S.u[1][2 * i + 1]),
          "=&v"(s0), "=&v"(s1), "=&v"(u0), "=&v"(u1)
        : "v"(v[0][2 * i]), "v"(v[0][2 * i + 1]), "v"(v[1][2 * i]), "v"(v[1][2 * i + 1]),
          "v"(st[i >> 1][2 * (i & 1)]), "v"(st[i >> 1][2 * (i & 1) + 1]),
          "v"(ar[0]), "v"(ar[1]), "v"(si[0]), "v"(si[1]), "v"(a0), "v"(a1));
    a0 = u0; a1 = u1;
    if (i == 0) {  } if (i == 1) {  } if (i == 2) {  } if (i == 3) {  } if (i == 4) {  } if (i == 5) {  } if (i == 6) {  } if (i == 7) {  }
  }
  float b0, b1;
  asm volatile("v_mul_f32 %0, %2, %4\n\tv_mul_f32 %1, %3, %5\n\t"
               "v_fmac_f32_dpp %0, %2, %6" DPPSW "\n\tv_fmac_f32_dpp %1, %3, %7" DPPSW
               : "=&v"(b0), "=&v"(b1) : "v"(a0), "v"(a1), "v"(S.k0[0]), "v"(S.k0[1]), "v"(S.k2[0]), "v"(S.k2[1]));
  lds_wf(M_AGG + ((2 * wm + h) * 32 + c32) * 4, b0);
  lds_wf(M_AGG + ((8 + 2 * wm + h) * 32 + c32) * 4, b1);
  unsigned ad = M_CNT, one = 1, ret;
  asm volatile("s_mov_b64 exec, 1\n\tds_add_rtn_u32 %0, %1, %2\n\ts_mov_b64 exec, -1\n\ts_waitcnt lgkmcnt(0)"
               : "=&v"(ret) : "v"(ad), "v"(one) : "memory");
  return __builtin_amdgcn_readfirstlane(ret);
}
template <int I> DEV void ssm_sc(Ssm& S) {
  float t0, t1, t2, t3;
  asm volatile(
      "v_mul_f32 %0, %4, %8\n\tv_mul_f32 %1, %5, %8\n\tv_mul_f32 %2, %6, %9\n\tv_mul_f32 %3, %7, %9\n\t"
      "v_fmac_f32_dpp %0, %4, %10" DPPSW "\n\tv_fmac_f32_dpp %1, %5, %10" DPPSW "\n\t"
      "v_fmac_f32_dpp %2, %6, %11" DPPSW "\n\tv_fmac_f32_dpp %3, %7, %11" DPPSW
      : "=&v"(t0), "=&v"(t1), "=&v"(t2), "=&v"(t3)
      : "v"(S.u[0][2 * I]), "v"(S.u[0][2 * I + 1]), "v"(S.u[1][2 * I]), "v"(S.u[1][2 * I + 1]),
        "v"(S.k0[0]), "v"(S.k0[1]), "v"(S.k2[0]), "v"(S.k2[1]));
  S.u[0][2 * I] = t0; S.u[0][2 * I + 1] = t1; S.u[1][2 * I] = t2; S.u[1][2 * I + 1] = t3;
}
DEV void ssm_e2(const Ctx& c) {
  int lane = c.lane;
  TIE(lane);
  int h = lane >> 5, c32 = lane & 31, part = lane & 1, s16 = c32 >> 1;
  f32x2 p = lds_rf2(M_PAR + (h * 16 + s16) * 48 + 16);
  float lr = p[0], sli = part ? p[1] : -p[1], b = 0.f, g[8];
  unsigned aa = M_AGG + (h * 256 + c32) * 4;
#pragma unroll
  for (int q = 0; q < 8; q++) g[q] = lds_rf(aa + q * 128);
  waitl();

#pragma unroll
  for (int q = 0; q < 8; q++) {
    if (q == 4) {  }
    lds_wf(aa + q * 128, b);
    float tt = __builtin_fmaf(lr, b, g[q]);
    b = __builtin_fmaf(sli, swp(b), tt);
  }

  float o = swp(b);
  u32x4 gr = granule(c, b, o);
  gst16sm((unsigned)((((2 * c.g + h) * 16 + c.mt) * 16 + s16) * 16), gr, c.ws + W_AGG, 0x5555555555555555ull);
  csig<16, 4>();
}
DEV void job_issue(const Ctx& c, int tl, u32x4 (&g)[4]) {
  int lane = c.lane;
  TIE(lane);
  int h = lane >> 5, c32 = lane & 31, part = lane & 1, s16 = c32 >> 1;
  int sub = part + 2 * h;
#pragma unroll
  for (int i = 0; i < 4; i++) {
    int j = sub + 4 * i;
    gld16s(g[i], (unsigned)((((2 * c.g + tl) * 16 + (j < c.mt ? j : 0)) * 16 + s16) * 16), c.ws + W_AGG);
  }
}
DEV void job_finish(const Ctx& c, int tl, u32x4 (&g)[4]) {
  int lane = c.lane;
  TIE(lane);
  int h = lane >> 5, c32 = lane & 31, part = lane & 1, s16 = c32 >> 1, mt = c.mt;
  int sub = part + 2 * h;
  cwaitx<4>(*(volatile AS3 unsigned*)(uintptr_t)(M_CNT + 4), 16u, 16u);
  if (mt > 0) {
    unsigned pa = M_PAR + (tl * 16 + s16) * 48;
    f32x2 p1 = lds_rf2(pa + 16), p2 = lds_rf2(pa + 32), wt[4];
    float pre[4];

    unsigned aa = M_AGG + ((tl * 8 + 4 * h) * 32 + c32) * 4;
#pragma unroll
    for (int i = 0; i < 4; i++) {
      int m = mt - 1 - sub - 4 * i;
      wt[i] = lds_rf2(M_PW + (tl * 16 + s16) * 128 + (m < 0 ? 15 : m) * 8);
    }
#pragma unroll
    for (int q = 0; q < 4; q++) pre[q] = lds_rf(aa + q * 128);

    int spins = 0;
    while (true) {
      bool ok = tag_ok(c, g[0]) && tag_ok(c, g[1]) && tag_ok(c, g[2]) && tag_ok(c, g[3]);
      if (__all(ok) || ++spins > SPIN_MAX) break;
      __builtin_amdgcn_s_sleep(1);
      job_issue(c, tl, g);
      asm volatile("s_waitcnt vmcnt(0)" : "+v"(g[0]), "+v"(g[1]), "+v"(g[2]), "+v"(g[3])::"memory");
    }

    float pr0 = 0.f, pi0 = 0.f, pr1 = 0.f, pi1 = 0.f;
#pragma unroll
    for (int i = 0; i < 4; i++) {
      float a = __uint_as_float(g[i][2]), b = __uint_as_float(g[i][3]);
      pr0 = fma1(a, wt[i][0], pr0); pr1 = fma1(-b, wt[i][1], pr1);
      pi0 = fma1(a, wt[i][1], pi0); pi1 = fma1(b, wt[i][0], pi1);
    }
    float pr = pr0 + pr1, pim = pi0 + pi1;
    TIE(pr); TIE(pim);
    pr += swp(pr); pim += swp(pim);
    pr = x32sum(pr); pim = x32sum(pim);
    float e0 = h ? p2[0] : 1.f, e1 = h ? p2[1] : 0.f;
    float H = part ? fma1(pr, e1, mul1(pim, e0)) : fma1(pr, e0, -mul1(pim, e1));

    float lr = p1[0], sli = part ? p1[1] : -p1[1];
#pragma unroll
    for (int q = 0; q < 4; q++) {
      lds_wf(aa + q * 128, pre[q] + H);
      H = __builtin_fmaf(sli, swp(H), lr * H);
    }
  }
  csig<1, 4>();
}
DEV void job_wait() { cwaitx<4>(*(volatile AS3 unsigned*)(uintptr_t)(M_CNT + 4), ~0u, 1024u + 18u); }
struct E3 { float ar[2], si[2], hc[2]; u32x4 o[2][2]; };
DEV void e3_pre(const Ctx& c, E3& z) {
  int lane = c.lane;
  TIE(lane);
  int h = lane >> 5, c32 = lane & 31, part = lane & 1, s16 = c32 >> 1, wm = c.wm;
#pragma unroll
  for (int t = 0; t < 2; t++) {
    f32x2 p = lds_rf2(M_PAR + (t * 16 + s16) * 48);
    z.ar[t] = p[0]; z.si[t] = part ? p[1] : -p[1];
    z.hc[t] = lds_rf(M_AGG + ((t * 8 + 2 * wm + h) * 32 + c32) * 4);
  }
}
template <int I> DEV void e3_st(const Ssm& S, E3& z) {
  float s0, s1, u0, u1;
  unsigned o0, o1;
  asm volatile(
      "v_fma_f32 %2, %6, %10, %12\n\t"
      "v_fma_f32 %3, %7, %11, %14\n\t"
      "v_fmac_f32_dpp %2, %10, %8" DPPSW "\n\t"
      "v_fmac_f32_dpp %3, %11, %9" DPPSW "\n\t"
      "v_fma_f32 %4, %6, %2, %13\n\t"
      "v_fma_f32 %5, %7, %3, %15\n\t"
      "v_fmac_f32_dpp %4, %2, %8" DPPSW "\n\t"
      "v_fmac_f32_dpp %5, %3, %9" DPPSW "\n\t"
      "v_cvt_pk_bf16_f32 %0, %2, %4\n\t"
      "v_cvt_pk_bf16_f32 %1, %3, %5"
      : "=&v"(o0), "=&v"(o1), "=&v"(s0), "=&v"(s1), "=&v"(u0), "=&v"(u1)
      : "v"(z.ar[0]), "v"(z.ar[1]), "v"(z.si[0]), "v"(z.si[1]), "v"(z.hc[0]), "v"(z.hc[1]),
        "v"(S.u[0][2 * I]), "v"(S.u[0][2 * I + 1]), "v"(S.u[1][2 * I]), "v"(S.u[1][2 * I + 1]));
  z.hc[0] = u0; z.hc[1] = u1;
  z.o[0][I >> 2][I & 3] = o0;
  z.o[1][I >> 2][I & 3] = o1;
}
DEV void e3_w(const Ctx& c, const E3& z) {
  int lane = c.lane;
  TIE(lane);
  unsigned wa = (unsigned)(SCR + c.wm * 8192 + ((lane & 1) * 16 + ((lane & 31) >> 1)) * 64 + (lane >> 5) * 32);
#pragma unroll
  for (int t = 0; t < 2; t++) {
    lds_w128(wa + t * 2048, z.o[t][0]);
    lds_w128(wa + t * 2048 + 16, z.o[t][1]);
  }
  asm volatile("" ::: "memory");
}
DEV void e3_r(const Ctx& c, u32x4 (&f)[4]) {
  int lane = c.lane;
  TIE(lane);
  int g16 = lane >> 4, a = g16 & 1, hh = g16 >> 1, q4 = (lane >> 2) & 3, p4 = lane & 3;
  unsigned ra = (unsigned)(SCR + c.wm * 8192 + (8 * hh + q4) * 64 + (16 * (p4 & 1) + 8 * a + 4 * (p4 >> 1)) * 2);
#pragma unroll
  for (int t = 0; t < 2; t++) {
    f[2 * t] = lds_tr2(ra + t * 2048, ra + t * 2048 + 256);
    f[2 * t + 1] = lds_tr2(ra + t * 2048 + 1024, ra + t * 2048 + 1024 + 256);
  }
}
DEV void e3_s(const Ctx& c, const u32x4 (&f)[4]) {
  int lane = c.lane;
  TIE(lane);
  char* hb = c.ws + W_H + (size_t)((4 * c.mt + c.wm) * 64 + (c.g >> 1) * 8 + (c.g & 1) * 2) * 1024;
#pragma unroll
  for (int t = 0; t < 2; t++) {
    gst16s((unsigned)(lane * 16 + t * 1024), f[2 * t], hb);
    gst16s((unsigned)(lane * 16 + t * 1024), f[2 * t + 1], hb + 4096);
  }
}
DEV unsigned flags_ok(const Ctx& c, const u32x4& f) {
  bool ok = f[0] == c.tag_lo && f[1] == c.tag_hi && f[2] == c.tag_lo && f[3] == c.tag_hi;
  return (unsigned)__ballot(ok);
}
DEV void wait_flags(const Ctx& c, unsigned fa, unsigned& ready, unsigned mask) {
  int spins = 0;
  while ((ready & mask) != mask && ++spins < SPIN_MAX) {
    u32x4 f;
    if (spins > 1) { __builtin_amdgcn_s_sleep(10); }
    gld16s(f, fa, c.ws);
    asm volatile("s_waitcnt vmcnt(0)" : "+v"(f)::"memory");
    ready = flags_ok(c, f);
  }
}

DEV unsigned flag8_ok(const Ctx& c, const u32x2& f) { return (unsigned)__ballot(f[0] == c.tag_lo && f[1] == c.tag_hi); }
DEV void pl8(u32x2& r, unsigned voff, const void* sbase) {
  asm volatile("global_load_dwordx2 %0, %1, %2 sc1" : "+v"(r) : "v"(voff), "s"(sbase));
}
#define PQ_STEP(a, b) pl8(f[a], fa, c.ws); asm volatile("s_waitcnt vmcnt(3)" : "+v"(f[b])::"memory"); ready = flag8_ok(c, f[b]); \
    if ((ready & mask) == mask) break; __builtin_amdgcn_s_sleep(4);
DEV void pollq(const Ctx& c, unsigned fa, unsigned& ready, unsigned mask, u32x2 (&f)[4]) {
  if ((ready & mask) != mask) {
    pl8(f[0], fa, c.ws); __builtin_amdgcn_s_sleep(4);
    pl8(f[1], fa, c.ws); __builtin_amdgcn_s_sleep(4);
    pl8(f[2], fa, c.ws); __builtin_amdgcn_s_sleep(4);
    for (int spins = 0; spins < SPIN_MAX; spins++) { PQ_STEP(3, 0) PQ_STEP(0, 1) PQ_STEP(1, 2) PQ_STEP(2, 3) }
  }
}
#define TS12 TS(12);
#define GSTOUT(a, v, b) gst16s(a, __builtin_bit_cast(u32x4, v), b)
#define CODE_BYTES 36000u

__global__ __launch_bounds__(512, 2) void s5_glu_fused(const char* x_, const char* lre_,
                                                        const char* lim_, const char* bre_,
                                                        const char* bim_, const char* cre_,
                                                        const char* cim_, const char* ldt_,
                                                        const char* dsk_, const char* wg_,
                                                        const char* wu_, const char* wd_,
                                                        char* out_, char* ws_) {
  __shared__ __attribute__((aligned(1024))) char smem[163840];
  TS(0);
  typedef __attribute__((ext_vector_type(16))) unsigned u32x16;
  typedef __attribute__((ext_vector_type(8))) unsigned u32x8;
  unsigned pfc, kd0, kd1;
  const unsigned long long kp = (unsigned long long)__builtin_amdgcn_kernarg_segment_ptr();
  {
    unsigned long long pc;
    asm volatile("s_load_dword %1, %3, 0x0\n\ts_load_dword %2, %3, 0x6c\n\ts_getpc_b64 %0" : "=&s"(pc), "=&s"(kd0), "=&s"(kd1) : "s"(kp));
    unsigned o = ((blockIdx.x >> 3) & 3) * 16384u + threadIdx.x * 32u;
    gld4(pfc, o < CODE_BYTES ? o : 0u, (const char*)(pc & ~0x7Full));
  }
  const int tid = threadIdx.x, lane = tid & 63;
  const int w = __builtin_amdgcn_readfirstlane(tid >> 6);
  const int bid = blockIdx.x, xcd = bid & 7, idx = bid >> 3;
  const int wm = w & 3, wn = w >> 2;
  asm volatile("" ::"v"((unsigned)(uintptr_t)(AS3 char*)smem));
  Ctx c;
  const int m8 = idx & 7, n4 = idx >> 3, ng = xcd >> 1;
  c.mt = (xcd & 1) * 8 + m8; c.w = w; c.wm = wm; c.lane = lane; c.g = ng * 4 + n4; c.bid = bid;
  c.rot = ((m8 + n4) & 3) * 2;
  c.q = 7 * ng + n4;
  c.u0 = 28 * ng + 16 + 3 * n4;
  c.mt64 = xcd * 4 + (idx & 3); c.nt = idx >> 2;
  c.rE = (c.nt + c.mt64) & 3; c.rL = (c.nt + 2 * (c.mt64 & 3)) & 7;
  const char *x, *dsk; char *out, *ws;
#define P64(v, i) ((char*)((unsigned long long)v[i] | (unsigned long long)v[i + 1] << 32))

  u32x2 xe[2], dk; u32x4 stg[2];
  unsigned tko[2];
#define EPI_LOADS { int l2 = c.lane; TIE(l2); \
    _Pragma("unroll") for (int i = 0; i < 2; i++) { \
      tko[i] = (unsigned)(64 * c.mt64 + (wn ? 32 : 0) + pi32(8 * wm + 4 * i + (l2 >> 4))); \
      gld8(xe[i], tko[i] * 1024 + (unsigned)((l2 & 15) * 8), x + c.nt * 128); \
      gld16s(stg[i], W_STAT + tko[i] * 16, ws); } \
    gld8(dk, (unsigned)((l2 & 15) * 8), dsk + c.nt * 128); }
#define EPI_WAIT { \
    asm volatile("s_waitcnt vmcnt(0)" : "+v"(xe[0]), "+v"(xe[1]), "+v"(dk), "+v"(stg[0]), "+v"(stg[1])::"memory"); \
    int spins = 0; \
    while (!__all(tag_ok(c, stg[0]) && tag_ok(c, stg[1])) && ++spins < SPIN_MAX) { \
      __builtin_amdgcn_s_sleep(1); \
      gld16s(stg[0], W_STAT + tko[0] * 16, ws); gld16s(stg[1], W_STAT + tko[1] * 16, ws); \
      asm volatile("s_waitcnt vmcnt(0)" : "+v"(stg[0]), "+v"(stg[1])::"memory"); } }
  {

  if (wn) {
  u32x16 ka; u32x8 kb_; u32x4 kc_;
  asm volatile("s_load_dwordx16 %0, %3, 0x0\n\ts_load_dwordx8 %1, %3, 0x40\n\ts_load_dwordx4 %2, %3, 0x60"
               : "=&s"(ka), "=&s"(kb_), "=&s"(kc_) : "s"(kp));
  int lane = c.lane; TIE(lane);
  unsigned vox0, vox1, voS, vog, vou;
  {
    int R0 = 16 * w + (lane >> 3), R1 = R0 + 8;
    vox0 = (unsigned)(4096 + (32 * (R0 >> 5) + pi32(R0 & 31)) * 1024 + (((lane & 7) ^ ((R0 >> 1) & 7)) * 16));
    vox1 = (unsigned)(3072 + (32 * (R1 >> 5) + pi32(R1 & 31)) * 1024 + (((lane & 7) ^ ((R1 >> 1) & 7)) * 16));
    int part = (w >> 1) & 1, s16 = 8 * (w & 1) + (lane >> 3);
    int f = ((s16 >> 1) & 3) | (part << 2);
    voS = (unsigned)(4096 + (16 * (2 * c.g + (w >> 2)) + s16) * 1024 + (((lane & 7) ^ f) * 16));
    int kk = 8 * w + (lane >> 3), up = (lane & 7) >> 1, sub = lane & 1, sw = (kk >> 1) & 1;
    vog = (unsigned)(3072 + kk * 3584 + (up ^ sw) * 32 + sub * 16);
    vou = (unsigned)(2048 + kk * 3584 + (up ^ (2 | sw)) * 32 + sub * 16);
  }
  asm volatile("s_waitcnt lgkmcnt(0)" : "+s"(ka), "+s"(kb_), "+s"(kc_), "+v"(vox0), "+v"(vox1), "+v"(voS), "+v"(vog), "+v"(vou) : "s"(kd0), "s"(kd1));
  x = P64(ka, 0); dsk = P64(kb_, 0);
  out = P64(kc_, 0); ws = P64(kc_, 2);
  c.x = x; c.lre = P64(ka, 2); c.lim = P64(ka, 4); c.bre = P64(ka, 6); c.bim = P64(ka, 8); c.cre = P64(ka, 10); c.cim = P64(ka, 12);
  c.ldt = P64(ka, 14); c.wg = P64(kb_, 2); c.wu = P64(kb_, 4); c.wd = P64(kb_, 6); c.ws = ws;
  c.xb = x + (size_t)c.mt * 131072;
  const u32x4 rX = mksrd(c.xb, 131072u), rS = mksrd(((w >> 1) & 1) ? c.bim : c.bre, 524288u);
  const u32x4 rG = mksrd(c.wg + c.q * 128, 1835008u - c.q * 128), rU = mksrd(c.wu + c.q * 128, 1835008u - c.q * 128);
  const unsigned w2k = w * 2048, w3k = w * 3072;
  unsigned k1 = c.rot * 128, k2 = c.rot * 229376;
  m0n<16384>(w2k); bl<0>(vox0, rX, k1);
  bl<1024>(vox1, rX, k1);
  m0n<49152>(w3k); bl<0>(voS, rS, k1);
  bl<1024>(vog, rG, k2);
  bl<2048>(vou, rU, k2);
  asm volatile("s_barrier" ::: "memory");
  u32x2 ep; u32x4 hx, hb, hw[2];
  const int mt = c.mt, blk = mt & 3, kq = mt >> 2, bst = (2 * c.g + (w & 1)) * 16 + mt;
  gld8s(ep, 0u, c.ws + W_EPOCH);
  { const char* wb = ((blk & 1) ? c.wu : c.wg) + ((blk >> 1) ? c.q * 128 : c.u0 * 32) + (unsigned)(128 * kq + 16 * w) * 3584u;
    int pc = lane & 7; pc = ((blk >> 1) || pc < 6) ? pc : 5;
    unsigned vo = (unsigned)((lane >> 3) * 3584 + pc * 16);
    gld16(hw[0], vo, wb); gld16(hw[1], vo, wb + 8 * 3584); }
  gld16(hx, (unsigned)(lane * 16), c.xb + (size_t)(8 * c.g + w) * 1024);
  gld16(hb, (unsigned)(lane * 16), ((w & 2) ? c.bim : c.bre) + (size_t)bst * 1024);
  m0k<32768, 128>(w2k, k1, k2); bl<0>(vox0, rX, k1);
  bl<1024>(vox1, rX, k1);
  m0n<73728>(w3k); bl<0>(voS, rS, k1);
  bl<1024>(vog, rG, k2);
  bl<2048>(vou, rU, k2);
  const int h = lane >> 5, c32 = lane & 31;
  unsigned pl0, pl1, pl2; float ppr, ppi;
  unsigned pf1 = 0;
  if (w == 0 && lane < 4) lds_wu(M_CNT + lane * 4, 0u);
  unsigned axs[4], aw[2][1], aS[2][4];
  {
    int R = 32 * wm + c32;
    unsigned ax = (unsigned)(R * 128 + ((h ^ ((R >> 1) & 7)) * 16));
    int g16 = lane >> 4, isU = g16 & 1, hh = g16 >> 1, q4 = (lane >> 2) & 3, p4 = lane & 3;
    int swz = ((q4 >> 1) & 1) | (isU << 1);
    unsigned b = (unsigned)(WR + 1024 + isU * 1024 + hh * 3072 + q4 * 128 + p4 * 8);
    int s16 = c32 >> 1, part = c32 & 1, f = ((s16 >> 1) & 3) | (part << 2);
    unsigned a1 = (unsigned)(WR + (2 * part + (s16 >> 3)) * 3072 + (s16 & 7) * 128 + ((h ^ f) * 16));
    axs[0] = ax ^ 0; TIE(axs[0]);
    axs[1] = ax ^ 32; TIE(axs[1]);
    axs[2] = ax ^ 64; TIE(axs[2]);
    axs[3] = ax ^ 96; TIE(axs[3]);
    aw[0][0] = b + (unsigned)((3 ^ swz) * 32); TIE(aw[0][0]);
    aS[0][0] = a1 ^ 0; TIE(aS[0][0]);
    aS[0][1] = a1 ^ 32; TIE(aS[0][1]);
    aS[0][2] = a1 ^ 64; TIE(aS[0][2]);
    aS[0][3] = a1 ^ 96; TIE(aS[0][3]);
  }
  u32x4 xf[32], wf[2][3], wt[8];
  f32x16 acc[3], accb[3];
#pragma unroll
  for (int t = 0; t < 3; t++)
#pragma unroll
    for (int i = 0; i < 16; i++) acc[t][i] = 0.f;
  unsigned voP[3], apl[2], aph[2];
  u32x4 rP;

  barv<10>();

  TS(1);
  xf[0] = lds_r128(axs[0] + 16384);

  wf[0][0] = lds_r128(aS[0][0] + 0); wf[0][1] = lds_r128(aS[0][0] + 12288);

  wf[0][2] = lds_tr2(aw[0][0] + 0, aw[0][0] + 512);

  SCHED();
  xf[1] = lds_r128(axs[1] + 16384);

  wf[1][0] = lds_r128(aS[0][1] + 0); wf[1][1] = lds_r128(aS[0][1] + 12288);

  wf[1][2] = lds_tr2(aw[0][0] + 6144, aw[0][0] + 6656);

  SCHED();
  acc[0] = mfma(xf[0], wf[0][0], acc[0]);

  SCHED();
  { unsigned vo = (unsigned)((c.g * 32 + c32) * 2);
    gldu16(pl0, vo, c.lre); gldu16(pl1, vo, c.lim); gldu16(pl2, vo, c.ldt); }

  SCHED();
  acc[1] = mfma(xf[0], wf[0][1], acc[1]);

  SCHED();
  m0k<0, 128>(w2k, k1, k2); bl<0>(vox0, rX, k1);

  SCHED();
  acc[2] = mfma(wf[0][2], xf[0], acc[2]);

  SCHED();
  bl<1024>(vox1, rX, k1);

  SCHED();
  xf[2] = lds_r128(axs[2] + 16384);

  wf[0][0] = lds_r128(aS[0][2] + 0); wf[0][1] = lds_r128(aS[0][2] + 12288);

  wf[0][2] = lds_tr2(aw[0][0] + 12288, aw[0][0] + 12800);

  SCHED();
  acc[0] = mfma(xf[1], wf[1][0], acc[0]);

  SCHED();
  m0n<98304>(w3k); bl<0>(voS, rS, k1);

  SCHED();
  acc[1] = mfma(xf[1], wf[1][1], acc[1]);

  SCHED();
  bl<1024>(vog, rG, k2);

  SCHED();
  acc[2] = mfma(wf[1][2], xf[1], acc[2]);

  SCHED();
  SCHED();
  TIE(acc[0]); TIE(acc[1]); TIE(acc[2]);
  asm volatile("s_waitcnt vmcnt(14)" : "+v"(hw[0]), "+v"(hw[1]), "+v"(ep), "+v"(pfc)::"memory");

  {
    int lane = c.lane; TIE(lane);
    unsigned slo = 0x3f80u, shi = 0x3f800000u; TIES(slo); TIES(shi);
    float v[8], a[4], b[2];
#pragma unroll
    for (int i = 0; i < 4; i++) {
      v[2 * i] = dot2(hw[1][i], slo, dot2(hw[0][i], slo, 0.f));
      v[2 * i + 1] = dot2(hw[1][i], shi, dot2(hw[0][i], shi, 0.f));
    }

#pragma unroll
    for (int i = 0; i < 4; i++) a[i] = sw32(v[i], v[4 + i]);

#pragma unroll
    for (int i = 0; i < 2; i++) b[i] = dppadd<0x128>(sw16(a[i], a[2 + i]));
    float t = (lane & 8) ? b[1] : b[0];
    int col = 8 * (lane & 7) + ((lane & 32) ? 4 : 0) + ((lane & 16) ? 2 : 0) + ((lane & 8) ? 1 : 0);

    lds_wf(M_PART + (col * 8 + w) * 4, t);
    unsigned long long ee = ((unsigned long long)ep[1] << 32 | ep[0]) + 1ull;
    c.tag_lo = __builtin_amdgcn_readfirstlane((unsigned)ee);
    c.tag_hi = __builtin_amdgcn_readfirstlane((unsigned)(ee >> 32));
  }

  SCHED();
  xf[3] = lds_r128(axs[3] + 16384);

  wf[1][0] = lds_r128(aS[0][3] + 0); wf[1][1] = lds_r128(aS[0][3] + 12288);

  wf[1][2] = lds_tr2(aw[0][0] + 18432, aw[0][0] + 18944);

  SCHED();
  acc[0] = mfma(xf[2], wf[0][0], acc[0]);

  SCHED();
  bl<2048>(vou, rU, k2);

  SCHED();
  acc[1] = mfma(xf[2], wf[0][1], acc[1]);

  SCHED();
  m0k<122880, 128>(w3k, k1, k2); bl<0>(voS, rS, k1);

  SCHED();
  acc[2] = mfma(wf[0][2], xf[2], acc[2]);

  SCHED();
  SCHED();
  TIE(acc[0]); TIE(acc[1]); TIE(acc[2]);
  asm volatile("s_waitcnt vmcnt(14)" : "+v"(hx), "+v"(hb)::"memory");

  {
    int lane = c.lane; TIE(lane);
    unsigned one = 0x3f803f80u; TIES(one);
    float s = 0.f, q = 0.f, b = 0.f;
#pragma unroll
    for (int i = 0; i < 4; i++) { s = dot2(hx[i], one, s); q = dot2(hx[i], hx[i], q); b = dot2(hb[i], one, b); }

    float v = sw16(sw32(s, q), sw32(b, b));
    v = dppadd<0xB1>(v); v = dppadd<0x4E>(v); v = dppadd<0x141>(v); v = dppadd<0x140>(v);
    float m = __uint_as_float(__builtin_amdgcn_readlane(__float_as_uint(v), 0)) * (1.f / 512.f);
    float qq = __uint_as_float(__builtin_amdgcn_readlane(__float_as_uint(v), 32)) * (1.f / 512.f);

    float r = __builtin_amdgcn_rsqf(__builtin_fmaxf(__builtin_fmaf(-m, m, qq), 0.f) + 1e-5f);
    u32x4 gr = granule(c, (lane & 16) ? v : m, r);
    unsigned ao = (lane & 16) ? W_CSB + (unsigned)((bst * 2 + ((w >> 1) & 1)) * 16) : W_STAT + (unsigned)((128 * mt + 8 * c.g + w) * 16);
    gst16sm(ao, gr, c.ws, 1ull);
  }

  SCHED();
  bl<1024>(vog, rG, k2);

  bl<2048>(vou, rU, k2);

  barv<12>();

  xf[4] = lds_r128(axs[0] + 32768);

  wf[0][0] = lds_r128(aS[0][0] + 24576); wf[0][1] = lds_r128(aS[0][0] + 36864);

  wf[0][2] = lds_tr2(aw[0][0] + 24576, aw[0][0] + 25088);

  SCHED();
  acc[0] = mfma(xf[3], wf[1][0], acc[0]);

  SCHED();
  m0n<16384>(w2k); bl<0>(vox0, rX, k1);

  SCHED();
  acc[1] = mfma(xf[3], wf[1][1], acc[1]);

  SCHED();
  bl<1024>(vox1, rX, k1);

  SCHED();
  acc[2] = mfma(wf[1][2], xf[3], acc[2]);

  SCHED();
  SCHED();
  xf[5] = lds_r128(axs[1] + 32768);

  wf[1][0] = lds_r128(aS[0][1] + 24576); wf[1][1] = lds_r128(aS[0][1] + 36864);

  wf[1][2] = lds_tr2(aw[0][0] + 30720, aw[0][0] + 31232);

  SCHED();
  acc[0] = mfma(xf[4], wf[0][0], acc[0]);

  SCHED();
  m0k<49152, 128>(w3k, k1, k2); bl<0>(voS, rS, k1);

  SCHED();
  acc[1] = mfma(xf[4], wf[0][1], acc[1]);

  SCHED();
  bl<1024>(vog, rG, k2);

  SCHED();
  acc[2] = mfma(wf[0][2], xf[4], acc[2]);

  SCHED();
  SCHED();
  TIE(acc[0]); TIE(acc[1]); TIE(acc[2]);
  if (wm == 3) {
    int lane = c.lane; TIE(lane);
    f32x4 a = lds_rf4(M_PART + lane * 32), b = lds_rf4(M_PART + lane * 32 + 16);
    float s = ((a[0] + a[1]) + (a[2] + a[3])) + ((b[0] + b[1]) + (b[2] + b[3]));
    TIE(s);

    float o = swp(s);
    u32x4 gr = granule(c, s, o);
    gst16sm((unsigned)((((c.g * 4 + blk) * 4 + kq) * 32 + (lane >> 1)) * 16), gr, c.ws + W_CSW, 0x5555555555555555ull);
  }

  SCHED();
  xf[6] = lds_r128(axs[2] + 32768);

  wf[0][0] = lds_r128(aS[0][2] + 24576); wf[0][1] = lds_r128(aS[0][2] + 36864);

  wf[0][2] = lds_tr2(aw[0][0] + 36864, aw[0][0] + 37376);

  SCHED();
  acc[0] = mfma(xf[5], wf[1][0], acc[0]);

  SCHED();
  bl<2048>(vou, rU, k2);

  SCHED();
  acc[1] = mfma(xf[5], wf[1][1], acc[1]);

  SCHED();
  SCHED();
  acc[2] = mfma(wf[1][2], xf[5], acc[2]);

  SCHED();
  SCHED();
  xf[7] = lds_r128(axs[3] + 32768);

  wf[1][0] = lds_r128(aS[0][3] + 24576); wf[1][1] = lds_r128(aS[0][3] + 36864);

  wf[1][2] = lds_tr2(aw[0][0] + 43008, aw[0][0] + 43520);

  SCHED();
  acc[0] = mfma(xf[6], wf[0][0], acc[0]);

  SCHED();
  SCHED();
  acc[1] = mfma(xf[6], wf[0][1], acc[1]);

  SCHED();
  SCHED();
  acc[2] = mfma(wf[0][2], xf[6], acc[2]);

  SCHED();
  SCHED();
  TIE(acc[0]); TIE(acc[1]); TIE(acc[2]);
  aw[1][0] = addk<49152>(aw[0][0]);

  aS[1][0] = addk<49152>(aS[0][0]);

  aS[1][1] = addk<49152>(aS[0][1]);

  aS[1][2] = addk<49152>(aS[0][2]);

  aS[1][3] = addk<49152>(aS[0][3]);

  SCHED();
  barv<9>();

  xf[8] = lds_r128(axs[0] + 0);

  wf[0][0] = lds_r128(aS[1][0] + 0); wf[0][1] = lds_r128(aS[1][0] + 12288);

  wf[0][2] = lds_tr2(aw[1][0] + 0, aw[1][0] + 512);

  SCHED();
  acc[0] = mfma(xf[7], wf[1][0], acc[0]);

  SCHED();
  m0n<32768>(w2k); bl<0>(vox0, rX, k1);

  SCHED();
  acc[1] = mfma(xf[7], wf[1][1], acc[1]);

  SCHED();
  bl<1024>(vox1, rX, k1);

  SCHED();
  acc[2] = mfma(wf[1][2], xf[7], acc[2]);

  SCHED();
  SCHED();
  xf[9] = lds_r128(axs[1] + 0);

  wf[1][0] = lds_r128(aS[1][1] + 0); wf[1][1] = lds_r128(aS[1][1] + 12288);

  wf[1][2] = lds_tr2(aw[1][0] + 6144, aw[1][0] + 6656);

  SCHED();
  acc[0] = mfma(xf[8], wf[0][0], acc[0]);

  SCHED();
  m0k<73728, 128>(w3k, k1, k2); bl<0>(voS, rS, k1);

  SCHED();
  acc[1] = mfma(xf[8], wf[0][1], acc[1]);

  SCHED();
  bl<1024>(vog, rG, k2);

  SCHED();
  acc[2] = mfma(wf[0][2], xf[8], acc[2]);

  SCHED();
  SCHED();
  TIE(acc[0]); TIE(acc[1]); TIE(acc[2]);
  asm volatile("s_waitcnt vmcnt(18)" : "+v"(pl0), "+v"(pl1), "+v"(pl2)::"memory");

  if (wm == 2) {
    float lr = bflo(pl0), li = bflo(pl1);
    float dt = (float)(__bf16)__expf(bflo(pl2));
    float ex = __builtin_amdgcn_exp2f(dt * lr * 1.4426950408889634f);
    float rev = __builtin_amdgcn_fractf(dt * li * 0.15915494309189535f);
    TIE(rev);
    float sn = __builtin_amdgcn_sinf(rev), cn = __builtin_amdgcn_cosf(rev);
    float ar = ex * cn, ai = ex * sn;
    float nr = ar - 1.f, ni = ai, den = 1.f / (lr * lr + li * li);
    TIE(den);
    float cr = (nr * lr + ni * li) * den, ci = (ni * lr - nr * li) * den;
    ppr = ar; ppi = -ai;
    f32x4 p0 = {ar, -ai, 2.f * cr, -2.f * ci};
    lds_wf4(M_PAR + c32 * 48, p0);
  }

  SCHED();
  xf[10] = lds_r128(axs[2] + 0);

  wf[0][0] = lds_r128(aS[1][2] + 0); wf[0][1] = lds_r128(aS[1][2] + 12288);

  wf[0][2] = lds_tr2(aw[1][0] + 12288, aw[1][0] + 12800);

  SCHED();
  acc[0] = mfma(xf[9], wf[1][0], acc[0]);

  SCHED();
  bl<2048>(vou, rU, k2);

  SCHED();
  acc[1] = mfma(xf[9], wf[1][1], acc[1]);

  SCHED();
  SCHED();
  acc[2] = mfma(wf[1][2], xf[9], acc[2]);

  SCHED();
  SCHED();
  xf[11] = lds_r128(axs[3] + 0);

  wf[1][0] = lds_r128(aS[1][3] + 0); wf[1][1] = lds_r128(aS[1][3] + 12288);

  wf[1][2] = lds_tr2(aw[1][0] + 18432, aw[1][0] + 18944);

  SCHED();
  acc[0] = mfma(xf[10], wf[0][0], acc[0]);

  SCHED();
  SCHED();
  acc[1] = mfma(xf[10], wf[0][1], acc[1]);

  SCHED();
  SCHED();
  acc[2] = mfma(wf[0][2], xf[10], acc[2]);

  SCHED();
  SCHED();
  barv<8>();

  xf[12] = lds_r128(axs[0] + 16384);

  wf[0][0] = lds_r128(aS[1][0] + 24576); wf[0][1] = lds_r128(aS[1][0] + 36864);

  wf[0][2] = lds_tr2(aw[1][0] + 24576, aw[1][0] + 25088);

  SCHED();
  acc[0] = mfma(xf[11], wf[1][0], acc[0]);

  SCHED();
  m0n<0>(w2k); bl<0>(vox0, rX, k1);

  SCHED();
  acc[1] = mfma(xf[11], wf[1][1], acc[1]);

  SCHED();
  bl<1024>(vox1, rX, k1);

  SCHED();
  acc[2] = mfma(wf[1][2], xf[11], acc[2]);

  SCHED();
  SCHED();
  xf[13] = lds_r128(axs[1] + 16384);

  wf[1][0] = lds_r128(aS[1][1] + 24576); wf[1][1] = lds_r128(aS[1][1] + 36864);

  wf[1][2] = lds_tr2(aw[1][0] + 30720, aw[1][0] + 31232);

  SCHED();
  acc[0] = mfma(xf[12], wf[0][0], acc[0]);

  SCHED();
  m0k<98304, 128>(w3k, k1, k2); bl<0>(voS, rS, k1);

  SCHED();
  acc[1] = mfma(xf[12], wf[0][1], acc[1]);

  SCHED();
  bl<1024>(vog, rG, k2);

  SCHED();
  acc[2] = mfma(wf[0][2], xf[12], acc[2]);

  SCHED();
  SCHED();
  TIE(acc[0]); TIE(acc[1]); TIE(acc[2]);
  if (wm == 2) {
    float sq[8];
#pragma unroll
    for (int i = 1; i <= 7; i++) {
      float a = fma1(ppr, ppr, -mul1(ppi, ppi)), b = mul1(ppr + ppr, ppi);
      ppr = a; ppi = b;
      if (i == 4) {  }
      if (i >= 4) { sq[2 * (i - 4)] = ppr; sq[2 * (i - 4) + 1] = ppi; }
    }
    f32x4 p1 = {sq[0], sq[1], sq[2], sq[3]}, p2 = {sq[4], sq[5], sq[6], sq[7]};
    lds_wf4(M_PAR + c32 * 48 + 16, p1); lds_wf4(M_PAR + c32 * 48 + 32, p2);
  }

  SCHED();
  xf[14] = lds_r128(axs[2] + 16384);

  wf[0][0] = lds_r128(aS[1][2] + 24576); wf[0][1] = lds_r128(aS[1][2] + 36864);

  wf[0][2] = lds_tr2(aw[1][0] + 36864, aw[1][0] + 37376);

  SCHED();
  acc[0] = mfma(xf[13], wf[1][0], acc[0]);

  SCHED();
  bl<2048>(vou, rU, k2);

  SCHED();
  acc[1] = mfma(xf[13], wf[1][1], acc[1]);

  SCHED();
  SCHED();
  acc[2] = mfma(wf[1][2], xf[13], acc[2]);

  SCHED();
  SCHED();
  TIE(acc[0]); TIE(acc[1]); TIE(acc[2]);
  {
    int lane = c.lane; TIE(lane);
#pragma unroll
    for (int j = 0; j < 3; j++) {
      int n = 64 * j + lane, rp = (n * 171) >> 10;
      voP[j] = (unsigned)((32 * wm + (rp ^ 4)) * 3584 + (n - 6 * rp) * 16 + 4096 - 1024 * j);
      TIE(voP[j]);
      if (j == 0) {  } if (j == 1) {  } if (j == 2) {  }
    }
    int g16 = lane >> 4, isU = g16 & 1, hh = g16 >> 1, q4 = (lane >> 2) & 3, p4 = lane & 3;
    unsigned b = (unsigned)(WR + isU * 12288 + (8 * hh + q4) * 96 + p4 * 8);
    apl[0] = b + isU * 384; aph[0] = b + 384 - isU * 384;
    TIE(apl[0]); TIE(aph[0]);
    rP = mksrd(c.wu + c.u0 * 32, 1835008u - c.u0 * 32);
  }

  SCHED();
  xf[15] = lds_r128(axs[3] + 16384);

  wf[1][0] = lds_r128(aS[1][3] + 24576); wf[1][1] = lds_r128(aS[1][3] + 36864);

  wf[1][2] = lds_tr2(aw[1][0] + 43008, aw[1][0] + 43520);

  SCHED();
  acc[0] = mfma(xf[14], wf[0][0], acc[0]);

  SCHED();
  SCHED();
  acc[1] = mfma(xf[14], wf[0][1], acc[1]);

  SCHED();
  SCHED();
  acc[2] = mfma(wf[0][2], xf[14], acc[2]);

  SCHED();
  SCHED();
  barv<8>();

  TS(2);
  xf[16] = lds_r128(axs[0] + 32768);

  wf[0][0] = lds_r128(aS[0][0] + 0); wf[0][1] = lds_r128(aS[0][0] + 12288);

  wf[0][2] = lds_tr2(aw[0][0] + 0, aw[0][0] + 512);

  SCHED();
  acc[0] = mfma(xf[15], wf[1][0], acc[0]);

  SCHED();
  m0n<16384>(w2k); bl<0>(vox0, rX, k1);

  SCHED();
  acc[1] = mfma(xf[15], wf[1][1], acc[1]);

  SCHED();
  bl<1024>(vox1, rX, k1);

  SCHED();
  acc[2] = mfma(wf[1][2], xf[15], acc[2]);

  SCHED();
  SCHED();
  xf[17] = lds_r128(axs[1] + 32768);

  wf[1][0] = lds_r128(aS[0][1] + 0); wf[1][1] = lds_r128(aS[0][1] + 12288);

  wf[1][2] = lds_tr2(aw[0][0] + 6144, aw[0][0] + 6656);

  SCHED();
  acc[0] = mfma(xf[16], wf[0][0], acc[0]);

  SCHED();
  m0k<122880, 128>(w3k, k1, k2); bl<0>(voS, rS, k1);

  SCHED();
  acc[1] = mfma(xf[16], wf[0][1], acc[1]);

  SCHED();
  bl<1024>(vog, rG, k2);

  SCHED();
  acc[2] = mfma(wf[0][2], xf[16], acc[2]);

  SCHED();
  SCHED();
  TIE(acc[0]); TIE(acc[1]); TIE(acc[2]);
  if (wm == 2) {
    float tr = ppr, ti = ppi;
#pragma unroll
    for (int i = 0; i < 3; i++) { float a = fma1(tr, tr, -mul1(ti, ti)), b = mul1(tr + tr, ti); tr = a; ti = b; }
    tr = h ? tr : 1.f; ti = h ? ti : 0.f;
#pragma unroll
    for (int i = 0; i < 4; i++) {
      float a = fma1(tr, ppr, -mul1(ti, ppi)), b = fma1(tr, ppi, mul1(ti, ppr));
      f32x4 o = {tr, ti, (i == 3 && h) ? 0.f : a, (i == 3 && h) ? 0.f : b};
      lds_wf4(M_PW + c32 * 128 + h * 64 + i * 16, o);
      if (i == 0) {  } if (i == 1) {  } if (i == 2) {  }
      if (i < 3) { tr = fma1(a, ppr, -mul1(b, ppi)); ti = fma1(a, ppi, mul1(b, ppr)); }
    }
  }

  SCHED();
  xf[18] = lds_r128(axs[2] + 32768);

  wf[0][0] = lds_r128(aS[0][2] + 0); wf[0][1] = lds_r128(aS[0][2] + 12288);

  wf[0][2] = lds_tr2(aw[0][0] + 12288, aw[0][0] + 12800);

  SCHED();
  acc[0] = mfma(xf[17], wf[1][0], acc[0]);

  SCHED();
  bl<2048>(vou, rU, k2);

  SCHED();
  acc[1] = mfma(xf[17], wf[1][1], acc[1]);

  SCHED();
  SCHED();
  acc[2] = mfma(wf[1][2], xf[17], acc[2]);

  SCHED();
  SCHED();
  TIE(acc[0]); TIE(acc[1]); TIE(acc[2]);
  unsigned vob, vob1, ab, kc, kc0;
  { int l2 = c.lane; TIE(l2); int rr = l2 >> 3; vob = (unsigned)(4096 + rr * 1024 + (((l2 & 7) ^ ((rr >> 1) & 3)) * 16));
ab = (unsigned)(wm * 8192 + (l2 & 31) * 128 + (((l2 >> 5) ^ ((l2 >> 1) & 7)) * 16)); vob1 = (vob ^ 64u) + 7168; }
  const u32x4 rC = mksrd(((wm & 1) ? c.cim : c.cre) + (size_t)c.nt * 65536 + (wm >> 1) * 128, 65536u - (wm >> 1) * 128);
  const unsigned w8k = wm * 8192;
  u32x4 cf[4][8];

  SCHED();
  xf[19] = lds_r128(axs[3] + 32768);

  wf[1][0] = lds_r128(aS[0][3] + 0); wf[1][1] = lds_r128(aS[0][3] + 12288);

  wf[1][2] = lds_tr2(aw[0][0] + 18432, aw[0][0] + 18944);

  SCHED();
  acc[0] = mfma(xf[18], wf[0][0], acc[0]);

  SCHED();
  SCHED();
  acc[1] = mfma(xf[18], wf[0][1], acc[1]);

  SCHED();
  SCHED();
  acc[2] = mfma(wf[0][2], xf[18], acc[2]);

  SCHED();
  SCHED();
  barv<8>();

  xf[20] = lds_r128(axs[0] + 0);

  wf[0][0] = lds_r128(aS[0][0] + 24576); wf[0][1] = lds_r128(aS[0][0] + 36864);

  wf[0][2] = lds_tr2(aw[0][0] + 24576, aw[0][0] + 25088);

  SCHED();
  acc[0] = mfma(xf[19], wf[1][0], acc[0]);

  SCHED();
  m0n<32768>(w2k); bl<0>(vox0, rX, k1);

  SCHED();
  acc[1] = mfma(xf[19], wf[1][1], acc[1]);

  SCHED();
  bl<1024>(vox1, rX, k1);

  SCHED();
  acc[2] = mfma(wf[1][2], xf[19], acc[2]);

  SCHED();
  SCHED();
  xf[21] = lds_r128(axs[1] + 0);

  wf[1][0] = lds_r128(aS[0][1] + 24576); wf[1][1] = lds_r128(aS[0][1] + 36864);

  wf[1][2] = lds_tr2(aw[0][0] + 30720, aw[0][0] + 31232);

  SCHED();
  acc[0] = mfma(xf[20], wf[0][0], acc[0]);

  SCHED();
  m0k<49152, 128>(w3k, k1, k2); bl<0>(voP[0], rP, k2);

  SCHED();
  acc[1] = mfma(xf[20], wf[0][1], acc[1]);

  SCHED();
  bl<1024>(voP[1], rP, k2);

  SCHED();
  acc[2] = mfma(wf[0][2], xf[20], acc[2]);

  SCHED();
  SCHED();
  TIE(acc[0]); TIE(acc[1]); TIE(acc[2]);
  u32x4 aux[1]; unsigned auxa;
  { int lane = c.lane; TIE(lane); auxa = W_STAT + (unsigned)((128 * mt + 32 * wm + pi32(lane & 31)) * 16); }
#define AUXLD1 gld16s(aux[0], auxa, c.ws);
  AUXLD1
  SCHED();
  xf[22] = lds_r128(axs[2] + 0);

  wf[0][0] = lds_r128(aS[0][2] + 24576); wf[0][1] = lds_r128(aS[0][2] + 36864);

  wf[0][2] = lds_tr2(aw[0][0] + 36864, aw[0][0] + 37376);

  SCHED();
  acc[0] = mfma(xf[21], wf[1][0], acc[0]);

  SCHED();
  bl<2048>(voP[2], rP, k2);

  SCHED();
  acc[1] = mfma(xf[21], wf[1][1], acc[1]);

  SCHED();
  SCHED();
  acc[2] = mfma(wf[1][2], xf[21], acc[2]);

  SCHED();
  SCHED();
  xf[23] = lds_r128(axs[3] + 0);

  wf[1][0] = lds_r128(aS[0][3] + 24576); wf[1][1] = lds_r128(aS[0][3] + 36864);

  wf[1][2] = lds_tr2(aw[0][0] + 43008, aw[0][0] + 43520);

  SCHED();
  acc[0] = mfma(xf[22], wf[0][0], acc[0]);

  SCHED();
  SCHED();
  acc[1] = mfma(xf[22], wf[0][1], acc[1]);

  SCHED();
  SCHED();
  acc[2] = mfma(wf[0][2], xf[22], acc[2]);

  SCHED();
  SCHED();
  barv<9>();

  xf[24] = lds_r128(axs[0] + 16384);

  wf[0][0] = lds_r128(aS[1][0] + 0); wf[0][1] = lds_r128(aS[1][0] + 12288);

  wf[0][2] = lds_tr2(aw[1][0] + 0, aw[1][0] + 512);

  SCHED();
  acc[0] = mfma(xf[23], wf[1][0], acc[0]);

  SCHED();
  m0k<73728, 256>(w3k, k1, k2); bl<0>(voP[0], rP, k2);

  SCHED();
  acc[1] = mfma(xf[23], wf[1][1], acc[1]);

  SCHED();
  bl<1024>(voP[1], rP, k2);

  SCHED();
  acc[2] = mfma(wf[1][2], xf[23], acc[2]);

  SCHED();
  SCHED();
  xf[25] = lds_r128(axs[1] + 16384);

  wf[1][0] = lds_r128(aS[1][1] + 0); wf[1][1] = lds_r128(aS[1][1] + 12288);

  wf[1][2] = lds_tr2(aw[1][0] + 6144, aw[1][0] + 6656);

  SCHED();
  acc[0] = mfma(xf[24], wf[0][0], acc[0]);

  SCHED();
  bl<2048>(voP[2], rP, k2);

  SCHED();
  acc[1] = mfma(xf[24], wf[0][1], acc[1]);

  SCHED();
  SCHED();
  acc[2] = mfma(wf[0][2], xf[24], acc[2]);

  SCHED();
  SCHED();
  xf[26] = lds_r128(axs[2] + 16384);

  wf[0][0] = lds_r128(aS[1][2] + 0); wf[0][1] = lds_r128(aS[1][2] + 12288);

  wf[0][2] = lds_tr2(aw[1][0] + 12288, aw[1][0] + 12800);

  SCHED();
  acc[0] = mfma(xf[25], wf[1][0], acc[0]);

  SCHED();
  SCHED();
  acc[1] = mfma(xf[25], wf[1][1], acc[1]);

  SCHED();
  SCHED();
  acc[2] = mfma(wf[1][2], xf[25], acc[2]);

  SCHED();
  SCHED();
  xf[27] = lds_r128(axs[3] + 16384);

  wf[1][0] = lds_r128(aS[1][3] + 0); wf[1][1] = lds_r128(aS[1][3] + 12288);

  wf[1][2] = lds_tr2(aw[1][0] + 18432, aw[1][0] + 18944);

  SCHED();
  acc[0] = mfma(xf[26], wf[0][0], acc[0]);

  SCHED();
  SCHED();
  acc[1] = mfma(xf[26], wf[0][1], acc[1]);

  SCHED();
  SCHED();
  acc[2] = mfma(wf[0][2], xf[26], acc[2]);

  SCHED();
  SCHED();
  barv<7>();

  xf[28] = lds_r128(axs[0] + 32768);

  wf[0][0] = lds_r128(aS[1][0] + 24576); wf[0][1] = lds_r128(aS[1][0] + 36864);

  wf[0][2] = lds_tr2(aw[1][0] + 24576, aw[1][0] + 25088);

  SCHED();
  acc[0] = mfma(xf[27], wf[1][0], acc[0]);

  SCHED();
  m0k<98304, 256>(w3k, k1, k2); bl<0>(voP[0], rP, k2);

  SCHED();
  acc[1] = mfma(xf[27], wf[1][1], acc[1]);

  SCHED();
  bl<1024>(voP[1], rP, k2);

  SCHED();
  acc[2] = mfma(wf[1][2], xf[27], acc[2]);

  SCHED();
  bl<2048>(voP[2], rP, k2);

  SCHED();
  xf[29] = lds_r128(axs[1] + 32768);

  wf[1][0] = lds_r128(aS[1][1] + 24576); wf[1][1] = lds_r128(aS[1][1] + 36864);

  wf[1][2] = lds_tr2(aw[1][0] + 30720, aw[1][0] + 31232);

  SCHED();
  acc[0] = mfma(xf[28], wf[0][0], acc[0]);

  SCHED();
  TIES(c.rE); kc0 = ((0 + c.rE) & 3) * 256; m0a<0, 0>(w8k, kc, kc0); bl<0>(vob, rC, kc);
  SCHED();
  acc[1] = mfma(xf[28], wf[0][1], acc[1]);

  SCHED();
  bl<1024>(vob1, rC, kc);

  SCHED();
  acc[2] = mfma(wf[0][2], xf[28], acc[2]);

  SCHED();
  sadd<14336>(kc, kc0); bl<2048>(vob, rC, kc);

  SCHED();
  xf[30] = lds_r128(axs[2] + 32768);

  wf[0][0] = lds_r128(aS[1][2] + 24576); wf[0][1] = lds_r128(aS[1][2] + 36864);

  wf[0][2] = lds_tr2(aw[1][0] + 36864, aw[1][0] + 37376);

  SCHED();
  acc[0] = mfma(xf[29], wf[1][0], acc[0]);

  SCHED();
  bl<3072>(vob1, rC, kc);

  SCHED();
  acc[1] = mfma(xf[29], wf[1][1], acc[1]);

  SCHED();
  m0a<4096, 32768>(w8k, kc, kc0); bl<0>(vob, rC, kc);

  SCHED();
  acc[2] = mfma(wf[1][2], xf[29], acc[2]);

  SCHED();
  SCHED();
  TIE(acc[0]); TIE(acc[1]); TIE(acc[2]);
  {
    int spins = 0;
    int lane = c.lane; TIE(lane);
    int h = lane >> 5, c32 = lane & 31;
    asm volatile("s_waitcnt vmcnt(12)" : "+v"(aux[0])::"memory");
    while (true) {
      bool ok = true;
#pragma unroll
      for (int i = 0; i < 1; i++) ok = ok && tag_ok(c, aux[i]);
      if (__all(ok) || ++spins > SPIN_MAX) break;
      __builtin_amdgcn_s_sleep(1);
      AUXLD1
      asm volatile("s_waitcnt vmcnt(0)" : "+v"(aux[0])::"memory");
    }

    if (h == 0) lds_w128(M_TF + (32 * wm + c32) * 16, frag_t(-__uint_as_float(aux[0][2])));
    else lds_wf(M_R + (32 * wm + pi32(c32)) * 4, __uint_as_float(aux[0][3]));
  }

  SCHED();
  xf[31] = lds_r128(axs[3] + 32768);

  wf[1][0] = lds_r128(aS[1][3] + 24576); wf[1][1] = lds_r128(aS[1][3] + 36864);

  wf[1][2] = lds_tr2(aw[1][0] + 43008, aw[1][0] + 43520);

  SCHED();
  acc[0] = mfma(xf[30], wf[0][0], acc[0]);

  SCHED();
  bl<1024>(vob1, rC, kc);

  SCHED();
  acc[1] = mfma(xf[30], wf[0][1], acc[1]);

  SCHED();
  sadd<47104>(kc, kc0); bl<2048>(vob, rC, kc);

  SCHED();
  acc[2] = mfma(wf[0][2], xf[30], acc[2]);

  SCHED();
  SCHED();
  bl<3072>(vob1, rC, kc);

  barv<11>();

  TS(3);
  __builtin_amdgcn_s_setprio(3);

  TIE(pf1);
  u32x4 tf, cfr[3];

  { int l2 = c.lane; TIE(l2); unsigned o = (unsigned)((l2 & 31) * 16); tf = lds_r128(M_TF + wm * 512 + o);

    cfr[0] = lds_r128(M_CS + o);

    cfr[1] = lds_r128(M_CS + 512 + o);

    cfr[2] = lds_r128(M_CF + 3584 + o);

  }

  SCHED();
  acc[0] = mfma(xf[31], wf[1][0], acc[0]);

  SCHED();
  m0k<122880, 256>(w3k, k1, k2); bl<0>(voP[0], rP, k2);

  SCHED();
  acc[1] = mfma(xf[31], wf[1][1], acc[1]);

  SCHED();
  bl<1024>(voP[1], rP, k2);

  SCHED();
  acc[2] = mfma(wf[1][2], xf[31], acc[2]);

  SCHED();
  SCHED();
  TIE(acc[0]); TIE(acc[1]); TIE(acc[2]);
  f32x16 z16;

  #pragma unroll
  for (int i = 0; i < 16; i++) z16[i] = 0.f;

  if (lane >> 5) { tf[0] = 0u; tf[1] = 0u; tf[2] = 0u; }

  acc[0] = mfma(tf, cfr[0], acc[0]);

  acc[1] = mfma(tf, cfr[1], acc[1]);

  acc[2] = mfma(cfr[2], tf, acc[2]);

  TIE(acc[0]); TIE(acc[1]); TIE(acc[2]);
  bl<2048>(voP[2], rP, k2);

  Ssm S; if (ssm_e1(c, acc, S) == 3u) ssm_e2(c);

  SCHED();
  __builtin_amdgcn_s_setprio(2);

  char* hgb = c.ws + W_HG + (size_t)(4 * mt + wm) * (112 * 1024);
  const unsigned flg = W_FLG + (unsigned)((mt * 16 + c.g) * 256 + wm * 8);
  const int mtp = c.mt64 >> 1, wmp = 2 * (c.mt64 & 1);
  float nr, ir;
  unsigned l16;
  { int l2 = c.lane; TIE(l2); float r = lds_rf(M_R + (32 * wm + pi32(l2 & 31)) * 4); l16 = (unsigned)(l2 * 16); TIE(l16);
    nr = -r * 1.4426950408889634f; ir = __builtin_amdgcn_rcpf(r); }

  TS(4);
  waitv<11>(); csig<1, 8>();

  ssm_sc<0>(S);

  ssm_sc<1>(S);

  ssm_sc<2>(S);

  ssm_sc<3>(S);

  ssm_sc<4>(S);

  ssm_sc<5>(S);

  ssm_sc<6>(S);

  ssm_sc<7>(S);

  waitv<3>();

  cf[0][0] = lds_r128(ab ^ 0); cf[0][1] = lds_r128((ab ^ 0) + 4096);

  cf[0][2] = lds_r128(ab ^ 32); cf[0][3] = lds_r128((ab ^ 32) + 4096);

  cf[0][4] = lds_r128(ab ^ 64); cf[0][5] = lds_r128((ab ^ 64) + 4096);

  cf[0][6] = lds_r128(ab ^ 96); cf[0][7] = lds_r128((ab ^ 96) + 4096);

  asm volatile("s_waitcnt lgkmcnt(0)" : "+v"(cf[0][0]), "+v"(cf[0][1]), "+v"(cf[0][2]), "+v"(cf[0][3]), "+v"(cf[0][4]), "+v"(cf[0][5]), "+v"(cf[0][6]), "+v"(cf[0][7])::"memory");

  TIES(c.rE); kc0 = ((1 + c.rE) & 3) * 256; m0a<0, 0>(w8k, kc, kc0); bl<0>(vob, rC, kc);
  bl<1024>(vob1, rC, kc);

  sadd<14336>(kc, kc0); bl<2048>(vob, rC, kc);

  bl<3072>(vob1, rC, kc);

  m0a<4096, 32768>(w8k, kc, kc0); bl<0>(vob, rC, kc);

  bl<1024>(vob1, rC, kc);

  sadd<47104>(kc, kc0); bl<2048>(vob, rC, kc);

  bl<3072>(vob1, rC, kc);

  Glu q13;

  glu_st<0>(acc[2], q13, nr, ir);

  glu_st<1>(acc[2], q13, nr, ir);

  glu_st<2>(acc[2], q13, nr, ir);

  glu_st<3>(acc[2], q13, nr, ir);

  glu_st<4>(acc[2], q13, nr, ir);

  glu_st<5>(acc[2], q13, nr, ir);

  gst16s(l16, q13.o, hgb + (c.q * 4 + 3) * 1024);

  waitv<9>(); csig<16, 8>();

  TS(5);
  u32x4 jg[4];

  job_issue(c, wm & 1, jg);

  asm volatile("s_waitcnt vmcnt(0)" : "+v"(jg[0]), "+v"(jg[1]), "+v"(jg[2]), "+v"(jg[3])::"memory");

  waitv<4>();

  set_flag(c, flg + 32);

  TS(6);
  waitv<6>();

  cf[1][0] = lds_r128(ab ^ 0); cf[1][1] = lds_r128((ab ^ 0) + 4096);

  cf[1][2] = lds_r128(ab ^ 32); cf[1][3] = lds_r128((ab ^ 32) + 4096);

  cf[1][4] = lds_r128(ab ^ 64); cf[1][5] = lds_r128((ab ^ 64) + 4096);

  cf[1][6] = lds_r128(ab ^ 96); cf[1][7] = lds_r128((ab ^ 96) + 4096);

  if (wm < 2) job_finish(c, wm, jg);

  job_wait();

  TS(7);
  asm volatile("s_waitcnt lgkmcnt(0)" : "+v"(cf[1][0]), "+v"(cf[1][1]), "+v"(cf[1][2]), "+v"(cf[1][3]), "+v"(cf[1][4]), "+v"(cf[1][5]), "+v"(cf[1][6]), "+v"(cf[1][7])::"memory");

  TIES(c.rE); kc0 = ((2 + c.rE) & 3) * 256; m0a<0, 0>(w8k, kc, kc0); bl<0>(vob, rC, kc);
  bl<1024>(vob1, rC, kc);

  sadd<14336>(kc, kc0); bl<2048>(vob, rC, kc);

  bl<3072>(vob1, rC, kc);

  m0a<4096, 32768>(w8k, kc, kc0); bl<0>(vob, rC, kc);

  bl<1024>(vob1, rC, kc);

  sadd<47104>(kc, kc0); bl<2048>(vob, rC, kc);

  bl<3072>(vob1, rC, kc);

  TS(13);
  E3 z; u32x4 ef[4];

  e3_pre(c, z);

  e3_st<0>(S, z);

  e3_st<1>(S, z);

  e3_st<2>(S, z);

  e3_st<3>(S, z);

  e3_st<4>(S, z);

  e3_st<5>(S, z);

  e3_st<6>(S, z);

  e3_st<7>(S, z);

  e3_w(c, z);

  e3_r(c, ef);

  e3_s(c, ef);

  unsigned fa, va1;
  {
    int l2 = c.lane; TIE(l2);
    int gg = 2 * ((wm >> 1) + 2 * (((l2 >> 1) + c.rE) & 3)) + (l2 & 1);
    fa = W_FLG + (unsigned)(mtp * 4096 + wmp * 8 + gg * 256 + 128);
    va1 = l16 + 65536;
  }
  const char* ha = c.ws + W_H + (size_t)(2 * c.mt64) * (64 * 1024) + wm * 4096;
  u32x4 A[4][8];
  unsigned ready = 0;

  waitv<0>();

  set_flag(c, flg + 128);

  __builtin_amdgcn_s_setprio(0);

  TS(8);
  waitv<5>();

  cf[2][0] = lds_r128(ab ^ 0); cf[2][1] = lds_r128((ab ^ 0) + 4096);

  cf[2][2] = lds_r128(ab ^ 32); cf[2][3] = lds_r128((ab ^ 32) + 4096);

  cf[2][4] = lds_r128(ab ^ 64); cf[2][5] = lds_r128((ab ^ 64) + 4096);

  cf[2][6] = lds_r128(ab ^ 96); cf[2][7] = lds_r128((ab ^ 96) + 4096);

  asm volatile("s_waitcnt lgkmcnt(0)" : "+v"(cf[2][0]), "+v"(cf[2][1]), "+v"(cf[2][2]), "+v"(cf[2][3]), "+v"(cf[2][4]), "+v"(cf[2][5]), "+v"(cf[2][6]), "+v"(cf[2][7])::"memory");

  TIES(c.rE); kc0 = ((3 + c.rE) & 3) * 256; m0a<0, 0>(w8k, kc, kc0); bl<0>(vob, rC, kc);
  bl<1024>(vob1, rC, kc);

  sadd<14336>(kc, kc0); bl<2048>(vob, rC, kc);

  bl<3072>(vob1, rC, kc);

  m0a<4096, 32768>(w8k, kc, kc0); bl<0>(vob, rC, kc);

  bl<1024>(vob1, rC, kc);

  sadd<47104>(kc, kc0); bl<2048>(vob, rC, kc);

  bl<3072>(vob1, rC, kc);

  if (c.bid == 0 && wm == 0) {
    int spins = 0;
    while (true) {
      u32x4 f0, f1;
      gld16s(f0, W_ARR + l16, c.ws); gld16s(f1, W_ARR + 1024 + l16, c.ws);
      asm volatile("s_waitcnt vmcnt(0)" : "+v"(f0), "+v"(f1)::"memory");
      bool ok = tag_ok(c, f0) && tag_ok(c, f1) && f0[2] == c.tag_lo && f0[3] == c.tag_hi && f1[2] == c.tag_lo && f1[3] == c.tag_hi;
      if (__all(ok) || ++spins > SPIN_MAX) break;
      __builtin_amdgcn_s_sleep(1);
    }
    set_flag(c, W_EPOCH);
  }

  f32x16 acc2[2][2];
#pragma unroll
  for (int a = 0; a < 2; a++)
#pragma unroll
    for (int b = 0; b < 2; b++)
#pragma unroll
      for (int i = 0; i < 16; i++) acc2[a][b][i] = 0.f;

  TIE(acc2[0][0]); TIE(acc2[0][1]); TIE(acc2[1][0]); TIE(acc2[1][1]);
  wait_flags(c, fa, ready, 0xff);

  TIES(c.rE); const char* ph0 = ha + ((0 + c.rE) & 3) * 16384; TIES(ph0);
  gld16so<0>(A[0][0], l16, ph0); gld16so<0>(A[0][1], va1, ph0);

  gld16so<1024>(A[0][2], l16, ph0); gld16so<1024>(A[0][3], va1, ph0);

  gld16so<2048>(A[0][4], l16, ph0); gld16so<2048>(A[0][5], va1, ph0);

  gld16so<3072>(A[0][6], l16, ph0); gld16so<3072>(A[0][7], va1, ph0);

  TIES(c.rE); const char* ph1 = ha + ((1 + c.rE) & 3) * 16384; TIES(ph1);
  gld16so<0>(A[1][0], l16, ph1); gld16so<0>(A[1][1], va1, ph1);

  gld16so<1024>(A[1][2], l16, ph1); gld16so<1024>(A[1][3], va1, ph1);

  gld16so<2048>(A[1][4], l16, ph1); gld16so<2048>(A[1][5], va1, ph1);

  gld16so<3072>(A[1][6], l16, ph1); gld16so<3072>(A[1][7], va1, ph1);

  TIES(c.rE); const char* ph2 = ha + ((2 + c.rE) & 3) * 16384; TIES(ph2);
  gld16so<0>(A[2][0], l16, ph2); gld16so<0>(A[2][1], va1, ph2);

  TS(14);
  asm volatile("s_waitcnt vmcnt(16)" : "+v"(A[0][0]), "+v"(A[0][1])::"memory");

  TS(9);
    acc2[0][0] = mfma(cf[0][0], A[0][0], acc2[0][0]); acc2[0][1] = mfma(cf[0][1], A[0][0], acc2[0][1]);

    acc2[1][0] = mfma(cf[0][0], A[0][1], acc2[1][0]); acc2[1][1] = mfma(cf[0][1], A[0][1], acc2[1][1]);

  TIE(acc2[0][0]); TIE(acc2[0][1]); TIE(acc2[1][0]); TIE(acc2[1][1]);
  gld16so<1024>(A[2][2], l16, ph2); gld16so<1024>(A[2][3], va1, ph2);

  asm volatile("s_waitcnt vmcnt(16)" : "+v"(A[0][2]), "+v"(A[0][3])::"memory");

    acc2[0][0] = mfma(cf[0][2], A[0][2], acc2[0][0]); acc2[0][1] = mfma(cf[0][3], A[0][2], acc2[0][1]);

    acc2[1][0] = mfma(cf[0][2], A[0][3], acc2[1][0]); acc2[1][1] = mfma(cf[0][3], A[0][3], acc2[1][1]);

  TIE(acc2[0][0]); TIE(acc2[0][1]); TIE(acc2[1][0]); TIE(acc2[1][1]);
  gld16so<2048>(A[2][4], l16, ph2); gld16so<2048>(A[2][5], va1, ph2);

  asm volatile("s_waitcnt vmcnt(16)" : "+v"(A[0][4]), "+v"(A[0][5])::"memory");

    acc2[0][0] = mfma(cf[0][4], A[0][4], acc2[0][0]); acc2[0][1] = mfma(cf[0][5], A[0][4], acc2[0][1]);

    acc2[1][0] = mfma(cf[0][4], A[0][5], acc2[1][0]); acc2[1][1] = mfma(cf[0][5], A[0][5], acc2[1][1]);

  TIE(acc2[0][0]); TIE(acc2[0][1]); TIE(acc2[1][0]); TIE(acc2[1][1]);
  gld16so<3072>(A[2][6], l16, ph2); gld16so<3072>(A[2][7], va1, ph2);

  TIES(c.rE); const char* ph3 = ha + ((3 + c.rE) & 3) * 16384; TIES(ph3);
  gld16so<0>(A[3][0], l16, ph3); gld16so<0>(A[3][1], va1, ph3);

  asm volatile("s_waitcnt vmcnt(18)" : "+v"(A[0][6]), "+v"(A[0][7])::"memory");

    acc2[0][0] = mfma(cf[0][6], A[0][6], acc2[0][0]); acc2[0][1] = mfma(cf[0][7], A[0][6], acc2[0][1]);

    acc2[1][0] = mfma(cf[0][6], A[0][7], acc2[1][0]); acc2[1][1] = mfma(cf[0][7], A[0][7], acc2[1][1]);

  TIE(acc2[0][0]); TIE(acc2[0][1]); TIE(acc2[1][0]); TIE(acc2[1][1]);
  gld16so<1024>(A[3][2], l16, ph3); gld16so<1024>(A[3][3], va1, ph3);

  gld16so<2048>(A[3][4], l16, ph3); gld16so<2048>(A[3][5], va1, ph3);

  asm volatile("s_waitcnt vmcnt(20)" : "+v"(A[1][0]), "+v"(A[1][1])::"memory");

    acc2[0][0] = mfma(cf[1][0], A[1][0], acc2[0][0]); acc2[0][1] = mfma(cf[1][1], A[1][0], acc2[0][1]);

    acc2[1][0] = mfma(cf[1][0], A[1][1], acc2[1][0]); acc2[1][1] = mfma(cf[1][1], A[1][1], acc2[1][1]);

  TIE(acc2[0][0]); TIE(acc2[0][1]); TIE(acc2[1][0]); TIE(acc2[1][1]);
  gld16so<3072>(A[3][6], l16, ph3); gld16so<3072>(A[3][7], va1, ph3);

  TS(10);
  EPI_LOADS
  asm volatile("s_waitcnt vmcnt(25)" : "+v"(A[1][2]), "+v"(A[1][3])::"memory");

    acc2[0][0] = mfma(cf[1][2], A[1][2], acc2[0][0]); acc2[0][1] = mfma(cf[1][3], A[1][2], acc2[0][1]);

    acc2[1][0] = mfma(cf[1][2], A[1][3], acc2[1][0]); acc2[1][1] = mfma(cf[1][3], A[1][3], acc2[1][1]);

  TIE(acc2[0][0]); TIE(acc2[0][1]); TIE(acc2[1][0]); TIE(acc2[1][1]);
  asm volatile("s_waitcnt vmcnt(23)" : "+v"(A[1][4]), "+v"(A[1][5])::"memory");

    acc2[0][0] = mfma(cf[1][4], A[1][4], acc2[0][0]); acc2[0][1] = mfma(cf[1][5], A[1][4], acc2[0][1]);

    acc2[1][0] = mfma(cf[1][4], A[1][5], acc2[1][0]); acc2[1][1] = mfma(cf[1][5], A[1][5], acc2[1][1]);

  TIE(acc2[0][0]); TIE(acc2[0][1]); TIE(acc2[1][0]); TIE(acc2[1][1]);
  asm volatile("s_waitcnt vmcnt(21)" : "+v"(A[1][6]), "+v"(A[1][7])::"memory");

    acc2[0][0] = mfma(cf[1][6], A[1][6], acc2[0][0]); acc2[0][1] = mfma(cf[1][7], A[1][6], acc2[0][1]);

    acc2[1][0] = mfma(cf[1][6], A[1][7], acc2[1][0]); acc2[1][1] = mfma(cf[1][7], A[1][7], acc2[1][1]);

  TIE(acc2[0][0]); TIE(acc2[0][1]); TIE(acc2[1][0]); TIE(acc2[1][1]);
  waitv<37>();

  cf[3][0] = lds_r128(ab ^ 0); cf[3][1] = lds_r128((ab ^ 0) + 4096);

  cf[3][2] = lds_r128(ab ^ 32); cf[3][3] = lds_r128((ab ^ 32) + 4096);

  cf[3][4] = lds_r128(ab ^ 64); cf[3][5] = lds_r128((ab ^ 64) + 4096);

  cf[3][6] = lds_r128(ab ^ 96); cf[3][7] = lds_r128((ab ^ 96) + 4096);

  asm volatile("s_waitcnt lgkmcnt(0)" : "+v"(cf[3][0]), "+v"(cf[3][1]), "+v"(cf[3][2]), "+v"(cf[3][3]), "+v"(cf[3][4]), "+v"(cf[3][5]), "+v"(cf[3][6]), "+v"(cf[3][7])::"memory");

  asm volatile("s_waitcnt vmcnt(19)" : "+v"(A[2][0]), "+v"(A[2][1])::"memory");

    acc2[0][0] = mfma(cf[2][0], A[2][0], acc2[0][0]); acc2[0][1] = mfma(cf[2][1], A[2][0], acc2[0][1]);

    acc2[1][0] = mfma(cf[2][0], A[2][1], acc2[1][0]); acc2[1][1] = mfma(cf[2][1], A[2][1], acc2[1][1]);

  TIE(acc2[0][0]); TIE(acc2[0][1]); TIE(acc2[1][0]); TIE(acc2[1][1]);
  asm volatile("s_waitcnt vmcnt(17)" : "+v"(A[2][2]), "+v"(A[2][3])::"memory");

    acc2[0][0] = mfma(cf[2][2], A[2][2], acc2[0][0]); acc2[0][1] = mfma(cf[2][3], A[2][2], acc2[0][1]);

    acc2[1][0] = mfma(cf[2][2], A[2][3], acc2[1][0]); acc2[1][1] = mfma(cf[2][3], A[2][3], acc2[1][1]);

  TIE(acc2[0][0]); TIE(acc2[0][1]); TIE(acc2[1][0]); TIE(acc2[1][1]);
  asm volatile("s_waitcnt vmcnt(15)" : "+v"(A[2][4]), "+v"(A[2][5])::"memory");

    acc2[0][0] = mfma(cf[2][4], A[2][4], acc2[0][0]); acc2[0][1] = mfma(cf[2][5], A[2][4], acc2[0][1]);

    acc2[1][0] = mfma(cf[2][4], A[2][5], acc2[1][0]); acc2[1][1] = mfma(cf[2][5], A[2][5], acc2[1][1]);

  TIE(acc2[0][0]); TIE(acc2[0][1]); TIE(acc2[1][0]); TIE(acc2[1][1]);
  asm volatile("s_waitcnt vmcnt(13)" : "+v"(A[2][6]), "+v"(A[2][7])::"memory");

    acc2[0][0] = mfma(cf[2][6], A[2][6], acc2[0][0]); acc2[0][1] = mfma(cf[2][7], A[2][6], acc2[0][1]);

    acc2[1][0] = mfma(cf[2][6], A[2][7], acc2[1][0]); acc2[1][1] = mfma(cf[2][7], A[2][7], acc2[1][1]);

  TIE(acc2[0][0]); TIE(acc2[0][1]); TIE(acc2[1][0]); TIE(acc2[1][1]);
  asm volatile("s_waitcnt vmcnt(11)" : "+v"(A[3][0]), "+v"(A[3][1])::"memory");

    acc2[0][0] = mfma(cf[3][0], A[3][0], acc2[0][0]); acc2[0][1] = mfma(cf[3][1], A[3][0], acc2[0][1]);

    acc2[1][0] = mfma(cf[3][0], A[3][1], acc2[1][0]); acc2[1][1] = mfma(cf[3][1], A[3][1], acc2[1][1]);

  TIE(acc2[0][0]); TIE(acc2[0][1]); TIE(acc2[1][0]); TIE(acc2[1][1]);
  asm volatile("s_waitcnt vmcnt(9)" : "+v"(A[3][2]), "+v"(A[3][3])::"memory");

    acc2[0][0] = mfma(cf[3][2], A[3][2], acc2[0][0]); acc2[0][1] = mfma(cf[3][3], A[3][2], acc2[0][1]);

    acc2[1][0] = mfma(cf[3][2], A[3][3], acc2[1][0]); acc2[1][1] = mfma(cf[3][3], A[3][3], acc2[1][1]);

  TIE(acc2[0][0]); TIE(acc2[0][1]); TIE(acc2[1][0]); TIE(acc2[1][1]);
  asm volatile("s_waitcnt vmcnt(7)" : "+v"(A[3][4]), "+v"(A[3][5])::"memory");

    acc2[0][0] = mfma(cf[3][4], A[3][4], acc2[0][0]); acc2[0][1] = mfma(cf[3][5], A[3][4], acc2[0][1]);

    acc2[1][0] = mfma(cf[3][4], A[3][5], acc2[1][0]); acc2[1][1] = mfma(cf[3][5], A[3][5], acc2[1][1]);

  TIE(acc2[0][0]); TIE(acc2[0][1]); TIE(acc2[1][0]); TIE(acc2[1][1]);
  asm volatile("s_waitcnt vmcnt(5)" : "+v"(A[3][6]), "+v"(A[3][7])::"memory");

    acc2[0][0] = mfma(cf[3][6], A[3][6], acc2[0][0]); acc2[0][1] = mfma(cf[3][7], A[3][6], acc2[0][1]);

    acc2[1][0] = mfma(cf[3][6], A[3][7], acc2[1][0]); acc2[1][1] = mfma(cf[3][7], A[3][7], acc2[1][1]);

  TIE(acc2[0][0]); TIE(acc2[0][1]); TIE(acc2[1][0]); TIE(acc2[1][1]);
  TS(11);
  cwaitx<4>(*(volatile AS3 unsigned*)(uintptr_t)(M_CNT + 4), 0xf00u, 0x400u);

  {
    int l2 = c.lane; TIE(l2);
    unsigned bw = (unsigned)(w8k + (l2 >> 5) * 512 + (((l2 & 31) ^ (l2 >> 5)) << 4));

    { unsigned a = bw ^ 0u; TIE(a);

      { f32x4 v = {acc2[0][0][0], acc2[0][0][1], acc2[0][0][2], acc2[0][0][3]}; lds_wf4(a + 0, v); }

      { f32x4 v = {acc2[1][0][0], acc2[1][0][1], acc2[1][0][2], acc2[1][0][3]}; lds_wf4(addk<122880>(a), v); }

    }

    { unsigned a = bw ^ 32u; TIE(a);

      { f32x4 v = {acc2[0][0][4], acc2[0][0][5], acc2[0][0][6], acc2[0][0][7]}; lds_wf4(a + 1024, v); }

      { f32x4 v = {acc2[1][0][4], acc2[1][0][5], acc2[1][0][6], acc2[1][0][7]}; lds_wf4(addk<123904>(a), v); }

    }

    { unsigned a = bw ^ 64u; TIE(a);

      { f32x4 v = {acc2[0][0][8], acc2[0][0][9], acc2[0][0][10], acc2[0][0][11]}; lds_wf4(a + 2048, v); }

      { f32x4 v = {acc2[1][0][8], acc2[1][0][9], acc2[1][0][10], acc2[1][0][11]}; lds_wf4(addk<124928>(a), v); }

    }

    { unsigned a = bw ^ 96u; TIE(a);

      { f32x4 v = {acc2[0][0][12], acc2[0][0][13], acc2[0][0][14], acc2[0][0][15]}; lds_wf4(a + 3072, v); }

      { f32x4 v = {acc2[1][0][12], acc2[1][0][13], acc2[1][0][14], acc2[1][0][15]}; lds_wf4(addk<125952>(a), v); }

    }

    { unsigned a = bw ^ 128u; TIE(a);

      { f32x4 v = {acc2[0][1][0], acc2[0][1][1], acc2[0][1][2], acc2[0][1][3]}; lds_wf4(a + 4096, v); }

      { f32x4 v = {acc2[1][1][0], acc2[1][1][1], acc2[1][1][2], acc2[1][1][3]}; lds_wf4(addk<126976>(a), v); }

    }

    { unsigned a = bw ^ 160u; TIE(a);

      { f32x4 v = {acc2[0][1][4], acc2[0][1][5], acc2[0][1][6], acc2[0][1][7]}; lds_wf4(a + 5120, v); }

      { f32x4 v = {acc2[1][1][4], acc2[1][1][5], acc2[1][1][6], acc2[1][1][7]}; lds_wf4(addk<128000>(a), v); }

    }

    { unsigned a = bw ^ 192u; TIE(a);

      { f32x4 v = {acc2[0][1][8], acc2[0][1][9], acc2[0][1][10], acc2[0][1][11]}; lds_wf4(a + 6144, v); }

      { f32x4 v = {acc2[1][1][8], acc2[1][1][9], acc2[1][1][10], acc2[1][1][11]}; lds_wf4(addk<129024>(a), v); }

    }

    { unsigned a = bw ^ 224u; TIE(a);

      { f32x4 v = {acc2[0][1][12], acc2[0][1][13], acc2[0][1][14], acc2[0][1][15]}; lds_wf4(a + 7168, v); }

      { f32x4 v = {acc2[1][1][12], acc2[1][1][13], acc2[1][1][14], acc2[1][1][15]}; lds_wf4(addk<130048>(a), v); }

    }

  }

  EPI_WAIT
  } else {
  u32x16 ka; u32x8 kb_; u32x4 kc_;
  asm volatile("s_load_dwordx16 %0, %3, 0x0\n\ts_load_dwordx8 %1, %3, 0x40\n\ts_load_dwordx4 %2, %3, 0x60"
               : "=&s"(ka), "=&s"(kb_), "=&s"(kc_) : "s"(kp));
  int lane = c.lane; TIE(lane);
  unsigned vox0, vox1, voS, vog, vou;
  {
    int R0 = 16 * w + (lane >> 3), R1 = R0 + 8;
    vox0 = (unsigned)(4096 + (32 * (R0 >> 5) + pi32(R0 & 31)) * 1024 + (((lane & 7) ^ ((R0 >> 1) & 7)) * 16));
    vox1 = (unsigned)(3072 + (32 * (R1 >> 5) + pi32(R1 & 31)) * 1024 + (((lane & 7) ^ ((R1 >> 1) & 7)) * 16));
    int part = (w >> 1) & 1, s16 = 8 * (w & 1) + (lane >> 3);
    int f = ((s16 >> 1) & 3) | (part << 2);
    voS = (unsigned)(4096 + (16 * (2 * c.g + (w >> 2)) + s16) * 1024 + (((lane & 7) ^ f) * 16));
    int kk = 8 * w + (lane >> 3), up = (lane & 7) >> 1, sub = lane & 1, sw = (kk >> 1) & 1;
    vog = (unsigned)(3072 + kk * 3584 + (up ^ sw) * 32 + sub * 16);
    vou = (unsigned)(2048 + kk * 3584 + (up ^ (2 | sw)) * 32 + sub * 16);
  }
  asm volatile("s_waitcnt lgkmcnt(0)" : "+s"(ka), "+s"(kb_), "+s"(kc_), "+v"(vox0), "+v"(vox1), "+v"(voS), "+v"(vog), "+v"(vou) : "s"(kd0), "s"(kd1));
  x = P64(ka, 0); dsk = P64(kb_, 0);
  out = P64(kc_, 0); ws = P64(kc_, 2);
  c.x = x; c.lre = P64(ka, 2); c.lim = P64(ka, 4); c.bre = P64(ka, 6); c.bim = P64(ka, 8); c.cre = P64(ka, 10); c.cim = P64(ka, 12);
  c.ldt = P64(ka, 14); c.wg = P64(kb_, 2); c.wu = P64(kb_, 4); c.wd = P64(kb_, 6); c.ws = ws;
  c.xb = x + (size_t)c.mt * 131072;
  const u32x4 rX = mksrd(c.xb, 131072u), rS = mksrd(((w >> 1) & 1) ? c.bim : c.bre, 524288u);
  const u32x4 rG = mksrd(c.wg + c.q * 128, 1835008u - c.q * 128), rU = mksrd(c.wu + c.q * 128, 1835008u - c.q * 128);
  const unsigned w2k = w * 2048, w3k = w * 3072;
  unsigned k1 = c.rot * 128, k2 = c.rot * 229376;
  m0n<16384>(w2k); bl<0>(vox0, rX, k1);
  bl<1024>(vox1, rX, k1);
  m0n<49152>(w3k); bl<0>(voS, rS, k1);
  bl<1024>(vog, rG, k2);
  bl<2048>(vou, rU, k2);
  asm volatile("s_barrier" ::: "memory");
  u32x2 ep; u32x4 hx, hb, hw[2];
  const int mt = c.mt, blk = mt & 3, kq = mt >> 2, bst = (2 * c.g + (w & 1)) * 16 + mt;
  gld8s(ep, 0u, c.ws + W_EPOCH);
  { const char* wb = ((blk & 1) ? c.wu : c.wg) + ((blk >> 1) ? c.q * 128 : c.u0 * 32) + (unsigned)(128 * kq + 16 * w) * 3584u;
    int pc = lane & 7; pc = ((blk >> 1) || pc < 6) ? pc : 5;
    unsigned vo = (unsigned)((lane >> 3) * 3584 + pc * 16);
    gld16(hw[0], vo, wb); gld16(hw[1], vo, wb + 8 * 3584); }
  gld16(hx, (unsigned)(lane * 16), c.xb + (size_t)(8 * c.g + w) * 1024);
  gld16(hb, (unsigned)(lane * 16), ((w & 2) ? c.bim : c.bre) + (size_t)bst * 1024);
  m0k<32768, 128>(w2k, k1, k2); bl<0>(vox0, rX, k1);
  bl<1024>(vox1, rX, k1);
  m0n<73728>(w3k); bl<0>(voS, rS, k1);
  bl<1024>(vog, rG, k2);
  bl<2048>(vou, rU, k2);
  const int h = lane >> 5, c32 = lane & 31;
  unsigned pf1 = 0;
  if (w == 0 && lane < 4) lds_wu(M_CNT + lane * 4, 0u);
  unsigned axs[4], aw[2][3], aS[2][4];
  {
    int R = 32 * wm + c32;
    unsigned ax = (unsigned)(R * 128 + ((h ^ ((R >> 1) & 7)) * 16));
    int g16 = lane >> 4, isU = g16 & 1, hh = g16 >> 1, q4 = (lane >> 2) & 3, p4 = lane & 3;
    int swz = ((q4 >> 1) & 1) | (isU << 1);
    unsigned b = (unsigned)(WR + 1024 + isU * 1024 + hh * 3072 + q4 * 128 + p4 * 8);
    int s16 = c32 >> 1, part = c32 & 1, f = ((s16 >> 1) & 3) | (part << 2);
    unsigned a1 = (unsigned)(WR + (2 * part + (s16 >> 3)) * 3072 + (s16 & 7) * 128 + ((h ^ f) * 16));
    axs[0] = ax ^ 0; TIE(axs[0]);
    axs[1] = ax ^ 32; TIE(axs[1]);
    axs[2] = ax ^ 64; TIE(axs[2]);
    axs[3] = ax ^ 96; TIE(axs[3]);
    aw[0][0] = b + (unsigned)((0 ^ swz) * 32); TIE(aw[0][0]);
    aw[0][1] = b + (unsigned)((1 ^ swz) * 32); TIE(aw[0][1]);
    aw[0][2] = b + (unsigned)((2 ^ swz) * 32); TIE(aw[0][2]);
  }
  u32x4 xf[32], wf[2][3], wt[8];
  f32x16 acc[3], accb[3];
#pragma unroll
  for (int t = 0; t < 3; t++)
#pragma unroll
    for (int i = 0; i < 16; i++) acc[t][i] = 0.f;
  unsigned voP[3], apl[2], aph[2];
  u32x4 rP;

  barv<10>();

  TS(1);
  xf[0] = lds_r128(axs[0] + 16384);

  wf[0][0] = lds_tr2(aw[0][0] + 0, aw[0][0] + 512);

  wf[0][1] = lds_tr2(aw[0][1] + 0, aw[0][1] + 512);

  wf[0][2] = lds_tr2(aw[0][2] + 0, aw[0][2] + 512);

  SCHED();
  xf[1] = lds_r128(axs[1] + 16384);

  wf[1][0] = lds_tr2(aw[0][0] + 6144, aw[0][0] + 6656);

  wf[1][1] = lds_tr2(aw[0][1] + 6144, aw[0][1] + 6656);

  wf[1][2] = lds_tr2(aw[0][2] + 6144, aw[0][2] + 6656);

  SCHED();
  acc[0] = mfma(wf[0][0], xf[0], acc[0]);

  SCHED();
  m0k<0, 128>(w2k, k1, k2); bl<0>(vox0, rX, k1);

  SCHED();
  acc[1] = mfma(wf[0][1], xf[0], acc[1]);

  SCHED();
  bl<1024>(vox1, rX, k1);

  SCHED();
  acc[2] = mfma(wf[0][2], xf[0], acc[2]);

  SCHED();
  m0n<98304>(w3k); bl<0>(voS, rS, k1);

  SCHED();
  xf[2] = lds_r128(axs[2] + 16384);

  wf[0][0] = lds_tr2(aw[0][0] + 12288, aw[0][0] + 12800);

  wf[0][1] = lds_tr2(aw[0][1] + 12288, aw[0][1] + 12800);

  wf[0][2] = lds_tr2(aw[0][2] + 12288, aw[0][2] + 12800);

  SCHED();
  acc[0] = mfma(wf[1][0], xf[1], acc[0]);

  SCHED();
  bl<1024>(vog, rG, k2);

  SCHED();
  acc[1] = mfma(wf[1][1], xf[1], acc[1]);

  SCHED();
  bl<2048>(vou, rU, k2);

  SCHED();
  acc[2] = mfma(wf[1][2], xf[1], acc[2]);

  SCHED();
  SCHED();
  TIE(acc[0]); TIE(acc[1]); TIE(acc[2]);
  asm volatile("s_waitcnt vmcnt(12)" : "+v"(hw[0]), "+v"(hw[1]), "+v"(ep), "+v"(pfc)::"memory");

  {
    int lane = c.lane; TIE(lane);
    unsigned slo = 0x3f80u, shi = 0x3f800000u; TIES(slo); TIES(shi);
    float v[8], a[4], b[2];
#pragma unroll
    for (int i = 0; i < 4; i++) {
      v[2 * i] = dot2(hw[1][i], slo, dot2(hw[0][i], slo, 0.f));
      v[2 * i + 1] = dot2(hw[1][i], shi, dot2(hw[0][i], shi, 0.f));
    }

#pragma unroll
    for (int i = 0; i < 4; i++) a[i] = sw32(v[i], v[4 + i]);

#pragma unroll
    for (int i = 0; i < 2; i++) b[i] = dppadd<0x128>(sw16(a[i], a[2 + i]));
    float t = (lane & 8) ? b[1] : b[0];
    int col = 8 * (lane & 7) + ((lane & 32) ? 4 : 0) + ((lane & 16) ? 2 : 0) + ((lane & 8) ? 1 : 0);

    lds_wf(M_PART + (col * 8 + w) * 4, t);
    unsigned long long ee = ((unsigned long long)ep[1] << 32 | ep[0]) + 1ull;
    c.tag_lo = __builtin_amdgcn_readfirstlane((unsigned)ee);
    c.tag_hi = __builtin_amdgcn_readfirstlane((unsigned)(ee >> 32));
  }

  SCHED();
  xf[3] = lds_r128(axs[3] + 16384);

  wf[1][0] = lds_tr2(aw[0][0] + 18432, aw[0][0] + 18944);

  wf[1][1] = lds_tr2(aw[0][1] + 18432, aw[0][1] + 18944);

  wf[1][2] = lds_tr2(aw[0][2] + 18432, aw[0][2] + 18944);

  SCHED();
  acc[0] = mfma(wf[0][0], xf[2], acc[0]);

  SCHED();
  m0k<122880, 128>(w3k, k1, k2); bl<0>(voS, rS, k1);

  SCHED();
  acc[1] = mfma(wf[0][1], xf[2], acc[1]);

  SCHED();
  bl<1024>(vog, rG, k2);

  SCHED();
  acc[2] = mfma(wf[0][2], xf[2], acc[2]);

  SCHED();
  SCHED();
  TIE(acc[0]); TIE(acc[1]); TIE(acc[2]);
  asm volatile("s_waitcnt vmcnt(12)" : "+v"(hx), "+v"(hb)::"memory");

  {
    int lane = c.lane; TIE(lane);
    unsigned one = 0x3f803f80u; TIES(one);
    float s = 0.f, q = 0.f, b = 0.f;
#pragma unroll
    for (int i = 0; i < 4; i++) { s = dot2(hx[i], one, s); q = dot2(hx[i], hx[i], q); b = dot2(hb[i], one, b); }

    float v = sw16(sw32(s, q), sw32(b, b));
    v = dppadd<0xB1>(v); v = dppadd<0x4E>(v); v = dppadd<0x141>(v); v = dppadd<0x140>(v);
    float m = __uint_as_float(__builtin_amdgcn_readlane(__float_as_uint(v), 0)) * (1.f / 512.f);
    float qq = __uint_as_float(__builtin_amdgcn_readlane(__float_as_uint(v), 32)) * (1.f / 512.f);

    float r = __builtin_amdgcn_rsqf(__builtin_fmaxf(__builtin_fmaf(-m, m, qq), 0.f) + 1e-5f);
    u32x4 gr = granule(c, (lane & 16) ? v : m, r);
    unsigned ao = (lane & 16) ? W_CSB + (unsigned)((bst * 2 + ((w >> 1) & 1)) * 16) : W_STAT + (unsigned)((128 * mt + 8 * c.g + w) * 16);
    gst16sm(ao, gr, c.ws, 0x10001ull);
  }

  SCHED();
  bl<2048>(vou, rU, k2);

  barv<9>();

  xf[4] = lds_r128(axs[0] + 32768);

  wf[0][0] = lds_tr2(aw[0][0] + 24576, aw[0][0] + 25088);

  wf[0][1] = lds_tr2(aw[0][1] + 24576, aw[0][1] + 25088);

  wf[0][2] = lds_tr2(aw[0][2] + 24576, aw[0][2] + 25088);

  SCHED();
  acc[0] = mfma(wf[1][0], xf[3], acc[0]);

  SCHED();
  m0n<16384>(w2k); bl<0>(vox0, rX, k1);

  SCHED();
  acc[1] = mfma(wf[1][1], xf[3], acc[1]);

  SCHED();
  bl<1024>(vox1, rX, k1);

  SCHED();
  acc[2] = mfma(wf[1][2], xf[3], acc[2]);

  SCHED();
  SCHED();
  xf[5] = lds_r128(axs[1] + 32768);

  wf[1][0] = lds_tr2(aw[0][0] + 30720, aw[0][0] + 31232);

  wf[1][1] = lds_tr2(aw[0][1] + 30720, aw[0][1] + 31232);

  wf[1][2] = lds_tr2(aw[0][2] + 30720, aw[0][2] + 31232);

  SCHED();
  acc[0] = mfma(wf[0][0], xf[4], acc[0]);

  SCHED();
  m0k<49152, 128>(w3k, k1, k2); bl<0>(voS, rS, k1);

  SCHED();
  acc[1] = mfma(wf[0][1], xf[4], acc[1]);

  SCHED();
  bl<1024>(vog, rG, k2);

  SCHED();
  acc[2] = mfma(wf[0][2], xf[4], acc[2]);

  SCHED();
  SCHED();
  xf[6] = lds_r128(axs[2] + 32768);

  wf[0][0] = lds_tr2(aw[0][0] + 36864, aw[0][0] + 37376);

  wf[0][1] = lds_tr2(aw[0][1] + 36864, aw[0][1] + 37376);

  wf[0][2] = lds_tr2(aw[0][2] + 36864, aw[0][2] + 37376);

  SCHED();
  acc[0] = mfma(wf[1][0], xf[5], acc[0]);

  SCHED();
  bl<2048>(vou, rU, k2);

  SCHED();
  acc[1] = mfma(wf[1][1], xf[5], acc[1]);

  SCHED();
  SCHED();
  acc[2] = mfma(wf[1][2], xf[5], acc[2]);

  SCHED();
  SCHED();
  TIE(acc[0]); TIE(acc[1]); TIE(acc[2]);
  if (w == 0) set_flag(c, W_ARR + c.bid * 8);

  SCHED();
  xf[7] = lds_r128(axs[3] + 32768);

  wf[1][0] = lds_tr2(aw[0][0] + 43008, aw[0][0] + 43520);

  wf[1][1] = lds_tr2(aw[0][1] + 43008, aw[0][1] + 43520);

  wf[1][2] = lds_tr2(aw[0][2] + 43008, aw[0][2] + 43520);

  SCHED();
  acc[0] = mfma(wf[0][0], xf[6], acc[0]);

  SCHED();
  SCHED();
  acc[1] = mfma(wf[0][1], xf[6], acc[1]);

  SCHED();
  SCHED();
  acc[2] = mfma(wf[0][2], xf[6], acc[2]);

  SCHED();
  SCHED();
  TIE(acc[0]); TIE(acc[1]); TIE(acc[2]);
  aw[1][0] = addk<49152>(aw[0][0]);

  aw[1][1] = addk<49152>(aw[0][1]);

  aw[1][2] = addk<49152>(aw[0][2]);

  SCHED();
  barv<9>();

  xf[8] = lds_r128(axs[0] + 0);

  wf[0][0] = lds_tr2(aw[1][0] + 0, aw[1][0] + 512);

  wf[0][1] = lds_tr2(aw[1][1] + 0, aw[1][1] + 512);

  wf[0][2] = lds_tr2(aw[1][2] + 0, aw[1][2] + 512);

  SCHED();
  acc[0] = mfma(wf[1][0], xf[7], acc[0]);

  SCHED();
  m0n<32768>(w2k); bl<0>(vox0, rX, k1);

  SCHED();
  acc[1] = mfma(wf[1][1], xf[7], acc[1]);

  SCHED();
  bl<1024>(vox1, rX, k1);

  SCHED();
  acc[2] = mfma(wf[1][2], xf[7], acc[2]);

  SCHED();
  SCHED();
  xf[9] = lds_r128(axs[1] + 0);

  wf[1][0] = lds_tr2(aw[1][0] + 6144, aw[1][0] + 6656);

  wf[1][1] = lds_tr2(aw[1][1] + 6144, aw[1][1] + 6656);

  wf[1][2] = lds_tr2(aw[1][2] + 6144, aw[1][2] + 6656);

  SCHED();
  acc[0] = mfma(wf[0][0], xf[8], acc[0]);

  SCHED();
  m0k<73728, 128>(w3k, k1, k2); bl<0>(voS, rS, k1);

  SCHED();
  acc[1] = mfma(wf[0][1], xf[8], acc[1]);

  SCHED();
  bl<1024>(vog, rG, k2);

  SCHED();
  acc[2] = mfma(wf[0][2], xf[8], acc[2]);

  SCHED();
  SCHED();
  xf[10] = lds_r128(axs[2] + 0);

  wf[0][0] = lds_tr2(aw[1][0] + 12288, aw[1][0] + 12800);

  wf[0][1] = lds_tr2(aw[1][1] + 12288, aw[1][1] + 12800);

  wf[0][2] = lds_tr2(aw[1][2] + 12288, aw[1][2] + 12800);

  SCHED();
  acc[0] = mfma(wf[1][0], xf[9], acc[0]);

  SCHED();
  bl<2048>(vou, rU, k2);

  SCHED();
  acc[1] = mfma(wf[1][1], xf[9], acc[1]);

  SCHED();
  SCHED();
  acc[2] = mfma(wf[1][2], xf[9], acc[2]);

  SCHED();
  SCHED();
  xf[11] = lds_r128(axs[3] + 0);

  wf[1][0] = lds_tr2(aw[1][0] + 18432, aw[1][0] + 18944);

  wf[1][1] = lds_tr2(aw[1][1] + 18432, aw[1][1] + 18944);

  wf[1][2] = lds_tr2(aw[1][2] + 18432, aw[1][2] + 18944);

  SCHED();
  acc[0] = mfma(wf[0][0], xf[10], acc[0]);

  SCHED();
  SCHED();
  acc[1] = mfma(wf[0][1], xf[10], acc[1]);

  SCHED();
  SCHED();
  acc[2] = mfma(wf[0][2], xf[10], acc[2]);

  SCHED();
  SCHED();
  barv<8>();

  xf[12] = lds_r128(axs[0] + 16384);

  wf[0][0] = lds_tr2(aw[1][0] + 24576, aw[1][0] + 25088);

  wf[0][1] = lds_tr2(aw[1][1] + 24576, aw[1][1] + 25088);

  wf[0][2] = lds_tr2(aw[1][2] + 24576, aw[1][2] + 25088);

  SCHED();
  acc[0] = mfma(wf[1][0], xf[11], acc[0]);

  SCHED();
  m0n<0>(w2k); bl<0>(vox0, rX, k1);

  SCHED();
  acc[1] = mfma(wf[1][1], xf[11], acc[1]);

  SCHED();
  bl<1024>(vox1, rX, k1);

  SCHED();
  acc[2] = mfma(wf[1][2], xf[11], acc[2]);

  SCHED();
  SCHED();
  xf[13] = lds_r128(axs[1] + 16384);

  wf[1][0] = lds_tr2(aw[1][0] + 30720, aw[1][0] + 31232);

  wf[1][1] = lds_tr2(aw[1][1] + 30720, aw[1][1] + 31232);

  wf[1][2] = lds_tr2(aw[1][2] + 30720, aw[1][2] + 31232);

  SCHED();
  acc[0] = mfma(wf[0][0], xf[12], acc[0]);

  SCHED();
  m0k<98304, 128>(w3k, k1, k2); bl<0>(voS, rS, k1);

  SCHED();
  acc[1] = mfma(wf[0][1], xf[12], acc[1]);

  SCHED();
  bl<1024>(vog, rG, k2);

  SCHED();
  acc[2] = mfma(wf[0][2], xf[12], acc[2]);

  SCHED();
  SCHED();
  xf[14] = lds_r128(axs[2] + 16384);

  wf[0][0] = lds_tr2(aw[1][0] + 36864, aw[1][0] + 37376);

  wf[0][1] = lds_tr2(aw[1][1] + 36864, aw[1][1] + 37376);

  wf[0][2] = lds_tr2(aw[1][2] + 36864, aw[1][2] + 37376);

  SCHED();
  acc[0] = mfma(wf[1][0], xf[13], acc[0]);

  SCHED();
  bl<2048>(vou, rU, k2);

  SCHED();
  acc[1] = mfma(wf[1][1], xf[13], acc[1]);

  SCHED();
  SCHED();
  acc[2] = mfma(wf[1][2], xf[13], acc[2]);

  SCHED();
  SCHED();
  TIE(acc[0]); TIE(acc[1]); TIE(acc[2]);
  {
    int lane = c.lane; TIE(lane);
#pragma unroll
    for (int j = 0; j < 3; j++) {
      int n = 64 * j + lane, rp = (n * 171) >> 10;
      voP[j] = (unsigned)((32 * wm + (rp ^ 0)) * 3584 + (n - 6 * rp) * 16 + 4096 - 1024 * j);
      TIE(voP[j]);
      if (j == 0) {  } if (j == 1) {  } if (j == 2) {  }
    }
    int g16 = lane >> 4, isU = g16 & 1, hh = g16 >> 1, q4 = (lane >> 2) & 3, p4 = lane & 3;
    unsigned b = (unsigned)(WR + isU * 12288 + (8 * hh + q4) * 96 + p4 * 8);
    apl[0] = b + isU * 384; aph[0] = b + 384 - isU * 384;
    TIE(apl[0]); TIE(aph[0]);
    rP = mksrd(c.wg + c.u0 * 32, 1835008u - c.u0 * 32);
  }

  SCHED();
  xf[15] = lds_r128(axs[3] + 16384);

  wf[1][0] = lds_tr2(aw[1][0] + 43008, aw[1][0] + 43520);

  wf[1][1] = lds_tr2(aw[1][1] + 43008, aw[1][1] + 43520);

  wf[1][2] = lds_tr2(aw[1][2] + 43008, aw[1][2] + 43520);

  SCHED();
  acc[0] = mfma(wf[0][0], xf[14], acc[0]);

  SCHED();
  SCHED();
  acc[1] = mfma(wf[0][1], xf[14], acc[1]);

  SCHED();
  SCHED();
  acc[2] = mfma(wf[0][2], xf[14], acc[2]);

  SCHED();
  SCHED();
  barv<8>();

  TS(2);
  xf[16] = lds_r128(axs[0] + 32768);

  wf[0][0] = lds_tr2(aw[0][0] + 0, aw[0][0] + 512);

  wf[0][1] = lds_tr2(aw[0][1] + 0, aw[0][1] + 512);

  wf[0][2] = lds_tr2(aw[0][2] + 0, aw[0][2] + 512);

  SCHED();
  acc[0] = mfma(wf[1][0], xf[15], acc[0]);

  SCHED();
  m0n<16384>(w2k); bl<0>(vox0, rX, k1);

  SCHED();
  acc[1] = mfma(wf[1][1], xf[15], acc[1]);

  SCHED();
  bl<1024>(vox1, rX, k1);

  SCHED();
  acc[2] = mfma(wf[1][2], xf[15], acc[2]);

  SCHED();
  SCHED();
  xf[17] = lds_r128(axs[1] + 32768);

  wf[1][0] = lds_tr2(aw[0][0] + 6144, aw[0][0] + 6656);

  wf[1][1] = lds_tr2(aw[0][1] + 6144, aw[0][1] + 6656);

  wf[1][2] = lds_tr2(aw[0][2] + 6144, aw[0][2] + 6656);

  SCHED();
  acc[0] = mfma(wf[0][0], xf[16], acc[0]);

  SCHED();
  m0k<122880, 128>(w3k, k1, k2); bl<0>(voS, rS, k1);

  SCHED();
  acc[1] = mfma(wf[0][1], xf[16], acc[1]);

  SCHED();
  bl<1024>(vog, rG, k2);

  SCHED();
  acc[2] = mfma(wf[0][2], xf[16], acc[2]);

  SCHED();
  SCHED();
  xf[18] = lds_r128(axs[2] + 32768);

  wf[0][0] = lds_tr2(aw[0][0] + 12288, aw[0][0] + 12800);

  wf[0][1] = lds_tr2(aw[0][1] + 12288, aw[0][1] + 12800);

  wf[0][2] = lds_tr2(aw[0][2] + 12288, aw[0][2] + 12800);

  SCHED();
  acc[0] = mfma(wf[1][0], xf[17], acc[0]);

  SCHED();
  bl<2048>(vou, rU, k2);

  SCHED();
  acc[1] = mfma(wf[1][1], xf[17], acc[1]);

  SCHED();
  SCHED();
  acc[2] = mfma(wf[1][2], xf[17], acc[2]);

  SCHED();
  SCHED();
  TIE(acc[0]); TIE(acc[1]); TIE(acc[2]);
  SCHED();
  xf[19] = lds_r128(axs[3] + 32768);

  wf[1][0] = lds_tr2(aw[0][0] + 18432, aw[0][0] + 18944);

  wf[1][1] = lds_tr2(aw[0][1] + 18432, aw[0][1] + 18944);

  wf[1][2] = lds_tr2(aw[0][2] + 18432, aw[0][2] + 18944);

  SCHED();
  acc[0] = mfma(wf[0][0], xf[18], acc[0]);

  SCHED();
  {
    int gw = c.bid * 4 + wm;
    int l2 = c.lane; TIE(l2);
    tch4(pf1, (unsigned)((gw * 14 + (l2 < 14 ? l2 : 0)) * 128), c.wd);
    tch4(pf1, (unsigned)(((gw >> 1) * 8 + (l2 & 7)) * 128), (gw & 1) ? c.cim : c.cre);
  }

  SCHED();
  acc[1] = mfma(wf[0][1], xf[18], acc[1]);

  SCHED();
  SCHED();
  acc[2] = mfma(wf[0][2], xf[18], acc[2]);

  SCHED();
  SCHED();
  barv<10>();

  xf[20] = lds_r128(axs[0] + 0);

  wf[0][0] = lds_tr2(aw[0][0] + 24576, aw[0][0] + 25088);

  wf[0][1] = lds_tr2(aw[0][1] + 24576, aw[0][1] + 25088);

  wf[0][2] = lds_tr2(aw[0][2] + 24576, aw[0][2] + 25088);

  SCHED();
  acc[0] = mfma(wf[1][0], xf[19], acc[0]);

  SCHED();
  m0n<32768>(w2k); bl<0>(vox0, rX, k1);

  SCHED();
  acc[1] = mfma(wf[1][1], xf[19], acc[1]);

  SCHED();
  bl<1024>(vox1, rX, k1);

  SCHED();
  acc[2] = mfma(wf[1][2], xf[19], acc[2]);

  SCHED();
  SCHED();
  xf[21] = lds_r128(axs[1] + 0);

  wf[1][0] = lds_tr2(aw[0][0] + 30720, aw[0][0] + 31232);

  wf[1][1] = lds_tr2(aw[0][1] + 30720, aw[0][1] + 31232);

  wf[1][2] = lds_tr2(aw[0][2] + 30720, aw[0][2] + 31232);

  SCHED();
  acc[0] = mfma(wf[0][0], xf[20], acc[0]);

  SCHED();
  m0k<49152, 128>(w3k, k1, k2); bl<0>(voP[0], rP, k2);

  SCHED();
  acc[1] = mfma(wf[0][1], xf[20], acc[1]);

  SCHED();
  bl<1024>(voP[1], rP, k2);

  SCHED();
  acc[2] = mfma(wf[0][2], xf[20], acc[2]);

  SCHED();
  SCHED();
  TIE(acc[0]); TIE(acc[1]); TIE(acc[2]);
  u32x4 aux[4]; unsigned auxa;
  {
    int lane = c.lane; TIE(lane);
    int h = lane >> 5, c32 = lane & 31;
    unsigned aW = W_CSW + (unsigned)((((c.g * 4 + (wm & 1) * 2 + h) * 4) * 32 + c32) * 16);
    unsigned aB = W_CSB + (unsigned)((c.g * 64 + 2 * c32 + h) * 16);
    auxa = wm < 2 ? aW : aB;
  }
  const char* auxb = c.ws + (wm < 2 ? 512 : 0);
#define AUXLD0 gld16s(aux[0], auxa, c.ws); gld16s(aux[1], auxa, auxb); \
    gld16s(aux[2], auxa, auxb + (wm < 2 ? 512 : 0)); gld16s(aux[3], auxa, auxb + (wm < 2 ? 1024 : 0));
  AUXLD0
  SCHED();
  xf[22] = lds_r128(axs[2] + 0);

  wf[0][0] = lds_tr2(aw[0][0] + 36864, aw[0][0] + 37376);

  wf[0][1] = lds_tr2(aw[0][1] + 36864, aw[0][1] + 37376);

  wf[0][2] = lds_tr2(aw[0][2] + 36864, aw[0][2] + 37376);

  SCHED();
  acc[0] = mfma(wf[1][0], xf[21], acc[0]);

  SCHED();
  bl<2048>(voP[2], rP, k2);

  SCHED();
  acc[1] = mfma(wf[1][1], xf[21], acc[1]);

  SCHED();
  SCHED();
  acc[2] = mfma(wf[1][2], xf[21], acc[2]);

  SCHED();
  SCHED();
  xf[23] = lds_r128(axs[3] + 0);

  wf[1][0] = lds_tr2(aw[0][0] + 43008, aw[0][0] + 43520);

  wf[1][1] = lds_tr2(aw[0][1] + 43008, aw[0][1] + 43520);

  wf[1][2] = lds_tr2(aw[0][2] + 43008, aw[0][2] + 43520);

  SCHED();
  acc[0] = mfma(wf[0][0], xf[22], acc[0]);

  SCHED();
  SCHED();
  acc[1] = mfma(wf[0][1], xf[22], acc[1]);

  SCHED();
  SCHED();
  acc[2] = mfma(wf[0][2], xf[22], acc[2]);

  SCHED();
  SCHED();
  barv<14>();

  xf[24] = lds_r128(axs[0] + 16384);

  wf[0][0] = lds_tr2(aw[1][0] + 0, aw[1][0] + 512);

  wf[0][1] = lds_tr2(aw[1][1] + 0, aw[1][1] + 512);

  wf[0][2] = lds_tr2(aw[1][2] + 0, aw[1][2] + 512);

  SCHED();
  acc[0] = mfma(wf[1][0], xf[23], acc[0]);

  SCHED();
  m0k<73728, 256>(w3k, k1, k2); bl<0>(voP[0], rP, k2);

  SCHED();
  acc[1] = mfma(wf[1][1], xf[23], acc[1]);

  SCHED();
  bl<1024>(voP[1], rP, k2);

  SCHED();
  acc[2] = mfma(wf[1][2], xf[23], acc[2]);

  SCHED();
  SCHED();
  xf[25] = lds_r128(axs[1] + 16384);

  wf[1][0] = lds_tr2(aw[1][0] + 6144, aw[1][0] + 6656);

  wf[1][1] = lds_tr2(aw[1][1] + 6144, aw[1][1] + 6656);

  wf[1][2] = lds_tr2(aw[1][2] + 6144, aw[1][2] + 6656);

  SCHED();
  acc[0] = mfma(wf[0][0], xf[24], acc[0]);

  SCHED();
  bl<2048>(voP[2], rP, k2);

  SCHED();
  acc[1] = mfma(wf[0][1], xf[24], acc[1]);

  SCHED();
  SCHED();
  acc[2] = mfma(wf[0][2], xf[24], acc[2]);

  SCHED();
  SCHED();
  xf[26] = lds_r128(axs[2] + 16384);

  wf[0][0] = lds_tr2(aw[1][0] + 12288, aw[1][0] + 12800);

  wf[0][1] = lds_tr2(aw[1][1] + 12288, aw[1][1] + 12800);

  wf[0][2] = lds_tr2(aw[1][2] + 12288, aw[1][2] + 12800);

  SCHED();
  acc[0] = mfma(wf[1][0], xf[25], acc[0]);

  SCHED();
  SCHED();
  acc[1] = mfma(wf[1][1], xf[25], acc[1]);

  SCHED();
  SCHED();
  acc[2] = mfma(wf[1][2], xf[25], acc[2]);

  SCHED();
  SCHED();
  xf[27] = lds_r128(axs[3] + 16384);

  wf[1][0] = lds_tr2(aw[1][0] + 18432, aw[1][0] + 18944);

  wf[1][1] = lds_tr2(aw[1][1] + 18432, aw[1][1] + 18944);

  wf[1][2] = lds_tr2(aw[1][2] + 18432, aw[1][2] + 18944);

  SCHED();
  acc[0] = mfma(wf[0][0], xf[26], acc[0]);

  SCHED();
  SCHED();
  acc[1] = mfma(wf[0][1], xf[26], acc[1]);

  SCHED();
  SCHED();
  acc[2] = mfma(wf[0][2], xf[26], acc[2]);

  SCHED();
  SCHED();
  barv<10>();

  xf[28] = lds_r128(axs[0] + 32768);

  wf[0][0] = lds_tr2(aw[1][0] + 24576, aw[1][0] + 25088);

  wf[0][1] = lds_tr2(aw[1][1] + 24576, aw[1][1] + 25088);

  wf[0][2] = lds_tr2(aw[1][2] + 24576, aw[1][2] + 25088);

  SCHED();
  acc[0] = mfma(wf[1][0], xf[27], acc[0]);

  SCHED();
  m0k<98304, 256>(w3k, k1, k2); bl<0>(voP[0], rP, k2);

  SCHED();
  acc[1] = mfma(wf[1][1], xf[27], acc[1]);

  SCHED();
  bl<1024>(voP[1], rP, k2);

  SCHED();
  acc[2] = mfma(wf[1][2], xf[27], acc[2]);

  SCHED();
  SCHED();
  xf[29] = lds_r128(axs[1] + 32768);

  wf[1][0] = lds_tr2(aw[1][0] + 30720, aw[1][0] + 31232);

  wf[1][1] = lds_tr2(aw[1][1] + 30720, aw[1][1] + 31232);

  wf[1][2] = lds_tr2(aw[1][2] + 30720, aw[1][2] + 31232);

  SCHED();
  acc[0] = mfma(wf[0][0], xf[28], acc[0]);

  SCHED();
  bl<2048>(voP[2], rP, k2);

  SCHED();
  acc[1] = mfma(wf[0][1], xf[28], acc[1]);

  SCHED();
  SCHED();
  acc[2] = mfma(wf[0][2], xf[28], acc[2]);

  SCHED();
  SCHED();
  xf[30] = lds_r128(axs[2] + 32768);

  wf[0][0] = lds_tr2(aw[1][0] + 36864, aw[1][0] + 37376);

  wf[0][1] = lds_tr2(aw[1][1] + 36864, aw[1][1] + 37376);

  wf[0][2] = lds_tr2(aw[1][2] + 36864, aw[1][2] + 37376);

  SCHED();
  acc[0] = mfma(wf[1][0], xf[29], acc[0]);

  SCHED();
  SCHED();
  acc[1] = mfma(wf[1][1], xf[29], acc[1]);

  SCHED();
  SCHED();
  acc[2] = mfma(wf[1][2], xf[29], acc[2]);

  SCHED();
  SCHED();
  TIE(acc[0]); TIE(acc[1]); TIE(acc[2]);
  {
    int spins = 0;
    int lane = c.lane; TIE(lane);
    int h = lane >> 5, c32 = lane & 31;
    asm volatile("s_waitcnt vmcnt(7)" : "+v"(aux[0]), "+v"(aux[1]), "+v"(aux[2]), "+v"(aux[3])::"memory");
    while (true) {
      bool ok = true;
#pragma unroll
      for (int i = 0; i < 4; i++) ok = ok && tag_ok(c, aux[i]);
      if (__all(ok) || ++spins > SPIN_MAX) break;
      __builtin_amdgcn_s_sleep(1);
      AUXLD0
      asm volatile("s_waitcnt vmcnt(0)" : "+v"(aux[0]), "+v"(aux[1]), "+v"(aux[2]), "+v"(aux[3])::"memory");
    }

    if (wm < 2) {
      float s0 = (__uint_as_float(aux[0][2]) + __uint_as_float(aux[1][2])) + (__uint_as_float(aux[2][2]) + __uint_as_float(aux[3][2]));
      TIE(s0);
      float s1 = (__uint_as_float(aux[0][3]) + __uint_as_float(aux[1][3])) + (__uint_as_float(aux[2][3]) + __uint_as_float(aux[3][3]));
      unsigned ca = M_CF + (unsigned)(((wm * 4 + (c32 >> 3)) * 32 + 16 * h + 2 * (c32 & 7)) * 16);
      lds_w128(ca, frag_c(s0));  lds_w128(ca + 16, frag_c(s1));
    } else if (wm == 2) {
      lds_w128(M_CS + (2 * c32 + h) * 16, frag_c(__uint_as_float(aux[0][2])));
    }
  }

  SCHED();
  xf[31] = lds_r128(axs[3] + 32768);

  wf[1][0] = lds_tr2(aw[1][0] + 43008, aw[1][0] + 43520);

  wf[1][1] = lds_tr2(aw[1][1] + 43008, aw[1][1] + 43520);

  wf[1][2] = lds_tr2(aw[1][2] + 43008, aw[1][2] + 43520);

  SCHED();
  acc[0] = mfma(wf[0][0], xf[30], acc[0]);

  SCHED();
  SCHED();
  acc[1] = mfma(wf[0][1], xf[30], acc[1]);

  SCHED();
  SCHED();
  acc[2] = mfma(wf[0][2], xf[30], acc[2]);

  SCHED();
  SCHED();
  barv<3>();

  TS(3);
  TIE(pf1);
  u32x4 tf, cfr[5];

  { int l2 = c.lane; TIE(l2); unsigned o = (unsigned)((l2 & 31) * 16); tf = lds_r128(M_TF + wm * 512 + o);

    cfr[0] = lds_r128(M_CF + 2048 + o);

    cfr[1] = lds_r128(M_CF + 2560 + o);

    cfr[2] = lds_r128(M_CF + 3072 + o);

    cfr[3] = lds_r128(M_CF + 0 + o);

    cfr[4] = lds_r128(M_CF + 512 + o);

  }

  wt[0] = lds_tr2(apl[0] + 0, aph[0] + 0); wt[1] = lds_tr2(apl[0] + 32, aph[0] + 32);

  SCHED();
  acc[0] = mfma(wf[1][0], xf[31], acc[0]);

  SCHED();
  m0k<122880, 256>(w3k, k1, k2); bl<0>(voP[0], rP, k2);

  SCHED();
  acc[1] = mfma(wf[1][1], xf[31], acc[1]);

  SCHED();
  bl<1024>(voP[1], rP, k2);

  SCHED();
  acc[2] = mfma(wf[1][2], xf[31], acc[2]);

  SCHED();
  SCHED();
  TIE(acc[0]); TIE(acc[1]); TIE(acc[2]);
  f32x16 z16;

  #pragma unroll
  for (int i = 0; i < 16; i++) z16[i] = 0.f;

  if (lane >> 5) { tf[0] = 0u; tf[1] = 0u; tf[2] = 0u; }

  acc[0] = mfma(cfr[0], tf, acc[0]);

  acc[1] = mfma(cfr[1], tf, acc[1]);

  acc[2] = mfma(cfr[2], tf, acc[2]);

  accb[0] = mfma(cfr[3], tf, z16);

  accb[1] = mfma(cfr[4], tf, z16);

  TIE(acc[0]); TIE(acc[1]); TIE(acc[2]);
  TIE(accb[0]); TIE(accb[1]);
  char* hgb = c.ws + W_HG + (size_t)(4 * mt + wm) * (112 * 1024);
  const unsigned flg = W_FLG + (unsigned)((mt * 16 + c.g) * 256 + wm * 8);
  const int mtp = c.mt64 >> 1, wmp = 2 * (c.mt64 & 1);
  float nr, ir;
  unsigned l16;
  { int l2 = c.lane; TIE(l2); float r = lds_rf(M_R + (32 * wm + pi32(l2 & 31)) * 4); l16 = (unsigned)(l2 * 16); TIE(l16);
    nr = -r * 1.4426950408889634f; ir = __builtin_amdgcn_rcpf(r); }

  unsigned vob, vob2, kb, kb0;
  { int l2 = c.lane; TIE(l2); int kk = l2 >> 3, pos = l2 & 7; vob = (unsigned)(4096 + kk * 1024 + ((pos ^ (((kk >> 1) & 1) << 2)) * 16)); vob2 = vob + 7168; }
  const u32x4 rD = mksrd(c.wd + c.nt * 128, 1835008u - c.nt * 128);
  const unsigned w4k = wm * 4096, w6k = wm * 6144, kE = wm * 65536, kL = 262144 + wm * 49152;

  apl[1] = addk<49152>(apl[0]); aph[1] = addk<49152>(aph[0]);

  unsigned fa, va1, ab[2][2], cv;
  const char* haE = c.ws + W_HG + (size_t)(2 * c.mt64) * (112 * 1024) + (4 * wm) * 1024;
  const char* haL = c.ws + W_HG + (size_t)(2 * c.mt64) * (112 * 1024) + (16 + 3 * wm) * 1024;
  u32x4 f, A[28][2], tf2, cf2;

  wt[2] = lds_tr2(apl[0] + 1536, aph[0] + 1536);

  SCHED();
  accb[0] = mfma(wt[0], xf[0], accb[0]);

  SCHED();
  bl<2048>(voP[2], rP, k2);

  wt[0] = lds_tr2(apl[0] + 1568, aph[0] + 1568);

  SCHED();
  accb[1] = mfma(wt[1], xf[0], accb[1]);

  SCHED();
  TIES(c.rE); kb0 = ((0 + c.rE) & 3) * 458752 + kE; m0a<32768, 0>(w4k, kb, kb0); bl<0>(vob, rD, kb);
  TIE(accb[0]); TIE(accb[1]);
  Glu q10;

  glu_st<0>(acc[0], q10, nr, ir);

  SCHED();
  wt[1] = lds_tr2(apl[0] + 3072, aph[0] + 3072);

  SCHED();
  accb[0] = mfma(wt[2], xf[1], accb[0]);

  SCHED();
  bl<1024>(vob2, rD, kb);

  wt[2] = lds_tr2(apl[0] + 3104, aph[0] + 3104);

  SCHED();
  accb[1] = mfma(wt[0], xf[1], accb[1]);

  SCHED();
  m0a<34816, 16384>(w4k, kb, kb0); bl<0>(vob, rD, kb);

  TIE(accb[0]); TIE(accb[1]);
  glu_st<1>(acc[0], q10, nr, ir);

  SCHED();
  wt[0] = lds_tr2(apl[0] + 4608, aph[0] + 4608);

  SCHED();
  accb[0] = mfma(wt[1], xf[2], accb[0]);

  SCHED();
  bl<1024>(vob2, rD, kb);

  wt[1] = lds_tr2(apl[0] + 4640, aph[0] + 4640);

  SCHED();
  accb[1] = mfma(wt[2], xf[2], accb[1]);

  SCHED();
  TIE(accb[0]); TIE(accb[1]);
  glu_st<2>(acc[0], q10, nr, ir);

  SCHED();
  wt[2] = lds_tr2(apl[0] + 6144, aph[0] + 6144);

  SCHED();
  accb[0] = mfma(wt[0], xf[3], accb[0]);

  SCHED();
  wt[0] = lds_tr2(apl[0] + 6176, aph[0] + 6176);

  SCHED();
  accb[1] = mfma(wt[1], xf[3], accb[1]);

  SCHED();
  TIE(accb[0]); TIE(accb[1]);
  glu_st<3>(acc[0], q10, nr, ir);

  SCHED();
  wt[1] = lds_tr2(apl[0] + 7680, aph[0] + 7680);

  SCHED();
  accb[0] = mfma(wt[2], xf[4], accb[0]);

  SCHED();
  wt[2] = lds_tr2(apl[0] + 7712, aph[0] + 7712);

  SCHED();
  accb[1] = mfma(wt[0], xf[4], accb[1]);

  SCHED();
  TIE(accb[0]); TIE(accb[1]);
  glu_st<4>(acc[0], q10, nr, ir);

  SCHED();
  wt[0] = lds_tr2(apl[0] + 9216, aph[0] + 9216);

  SCHED();
  accb[0] = mfma(wt[1], xf[5], accb[0]);

  SCHED();
  wt[1] = lds_tr2(apl[0] + 9248, aph[0] + 9248);

  SCHED();
  accb[1] = mfma(wt[2], xf[5], accb[1]);

  SCHED();
  TIE(accb[0]); TIE(accb[1]);
  glu_st<5>(acc[0], q10, nr, ir);

  gst16s(l16, q10.o, hgb + (c.q * 4 + 0) * 1024);

  SCHED();
  wt[2] = lds_tr2(apl[0] + 10752, aph[0] + 10752);

  SCHED();
  accb[0] = mfma(wt[0], xf[6], accb[0]);

  SCHED();
  TIE(accb[0]); TIE(accb[1]);
  tf2 = lds_r128(M_TF + wm * 512 + (l16 & 511u)); cf2 = lds_r128(M_CF + 1024 + (l16 & 511u));

  SCHED();
  wt[0] = lds_tr2(apl[0] + 10784, aph[0] + 10784);

  SCHED();
  accb[1] = mfma(wt[1], xf[6], accb[1]);

  SCHED();
  TIE(accb[0]); TIE(accb[1]);
  Glu q11;

  glu_st<0>(acc[1], q11, nr, ir);

  SCHED();
  wt[1] = lds_tr2(apl[0] + 64, aph[0] + 64);

  SCHED();
  accb[0] = mfma(wt[2], xf[7], accb[0]);

  SCHED();
  TIE(accb[0]); TIE(accb[1]);
  glu_st<1>(acc[1], q11, nr, ir);

  SCHED();
  wt[2] = lds_tr2(apl[0] + 1600, aph[0] + 1600);

  SCHED();
  accb[1] = mfma(wt[0], xf[7], accb[1]);

  SCHED();
  TIE(accb[0]); TIE(accb[1]);
  glu_st<2>(acc[1], q11, nr, ir);

  SCHED();
  if (l16 >> 9) { tf2[0] = 0u; tf2[1] = 0u; tf2[2] = 0u; }

  accb[2] = mfma(cf2, tf2, z16);

  SCHED();
  wt[0] = lds_tr2(apl[0] + 3136, aph[0] + 3136);

  SCHED();
  accb[2] = mfma(wt[1], xf[0], accb[2]);

  SCHED();
  TIE(accb[0]); TIE(accb[1]); TIE(accb[2]);
  glu_st<3>(acc[1], q11, nr, ir);

  SCHED();
  wt[1] = lds_tr2(apl[0] + 4672, aph[0] + 4672);

  SCHED();
  accb[2] = mfma(wt[2], xf[1], accb[2]);

  SCHED();
  TIE(accb[0]); TIE(accb[1]); TIE(accb[2]);
  glu_st<4>(acc[1], q11, nr, ir);

  SCHED();
  wt[2] = lds_tr2(apl[0] + 6208, aph[0] + 6208);

  SCHED();
  accb[2] = mfma(wt[0], xf[2], accb[2]);

  SCHED();
  TIE(accb[0]); TIE(accb[1]); TIE(accb[2]);
  glu_st<5>(acc[1], q11, nr, ir);

  gst16s(l16, q11.o, hgb + (c.q * 4 + 1) * 1024);

  SCHED();
  wt[0] = lds_tr2(apl[0] + 7744, aph[0] + 7744);

  SCHED();
  accb[2] = mfma(wt[1], xf[3], accb[2]);

  SCHED();
  TIE(accb[0]); TIE(accb[1]); TIE(accb[2]);
  Glu q12;

  glu_st<0>(acc[2], q12, nr, ir);

  SCHED();
  wt[1] = lds_tr2(apl[0] + 9280, aph[0] + 9280);

  SCHED();
  accb[2] = mfma(wt[2], xf[4], accb[2]);

  SCHED();
  TIE(accb[0]); TIE(accb[1]); TIE(accb[2]);
  glu_st<1>(acc[2], q12, nr, ir);

  SCHED();
  wt[2] = lds_tr2(apl[0] + 10816, aph[0] + 10816);

  SCHED();
  accb[2] = mfma(wt[0], xf[5], accb[2]);

  SCHED();
  TIE(accb[0]); TIE(accb[1]); TIE(accb[2]);
  glu_st<2>(acc[2], q12, nr, ir);

  SCHED();
  csig<256, 8>();

  SCHED();
  wt[0] = lds_tr2(apl[0] + 24576, aph[0] + 24576);

  SCHED();
  accb[2] = mfma(wt[1], xf[6], accb[2]);

  SCHED();
  TIE(accb[0]); TIE(accb[1]); TIE(accb[2]);
  glu_st<3>(acc[2], q12, nr, ir);

  SCHED();
  wt[1] = lds_tr2(apl[0] + 24608, aph[0] + 24608);

  SCHED();
  accb[2] = mfma(wt[2], xf[7], accb[2]);

  SCHED();
  TIE(accb[0]); TIE(accb[1]); TIE(accb[2]);
  glu_st<4>(acc[2], q12, nr, ir);

  SCHED();
  TS(4);
  wt[2] = lds_tr2(apl[0] + 24640, aph[0] + 24640);

  SCHED();
  accb[0] = mfma(wt[0], xf[8], accb[0]);

  SCHED();
  TIE(accb[0]); TIE(accb[1]); TIE(accb[2]);
  glu_st<5>(acc[2], q12, nr, ir);

  gst16s(l16, q12.o, hgb + (c.q * 4 + 2) * 1024);

  SCHED();
  wt[0] = lds_tr2(apl[0] + 26112, aph[0] + 26112);

  SCHED();
  accb[1] = mfma(wt[1], xf[8], accb[1]);

  SCHED();
  wt[1] = lds_tr2(apl[0] + 26144, aph[0] + 26144);

  SCHED();
  accb[2] = mfma(wt[2], xf[8], accb[2]);

  SCHED();
  wt[2] = lds_tr2(apl[0] + 26176, aph[0] + 26176);

  SCHED();
  accb[0] = mfma(wt[0], xf[9], accb[0]);

  SCHED();
  wt[0] = lds_tr2(apl[0] + 27648, aph[0] + 27648);

  SCHED();
  accb[1] = mfma(wt[1], xf[9], accb[1]);

  SCHED();
  wt[1] = lds_tr2(apl[0] + 27680, aph[0] + 27680);

  SCHED();
  accb[2] = mfma(wt[2], xf[9], accb[2]);

  SCHED();
  wt[2] = lds_tr2(apl[0] + 27712, aph[0] + 27712);

  SCHED();
  accb[0] = mfma(wt[0], xf[10], accb[0]);

  SCHED();
  TIE(accb[0]); TIE(accb[1]); TIE(accb[2]);
  waitv<10>(); csig<1, 8>();

  SCHED();
  wt[0] = lds_tr2(apl[0] + 29184, aph[0] + 29184);

  SCHED();
  accb[1] = mfma(wt[1], xf[10], accb[1]);

  SCHED();
  wt[1] = lds_tr2(apl[0] + 29216, aph[0] + 29216);

  SCHED();
  accb[2] = mfma(wt[2], xf[10], accb[2]);

  SCHED();
  wt[2] = lds_tr2(apl[0] + 29248, aph[0] + 29248);

  SCHED();
  accb[0] = mfma(wt[0], xf[11], accb[0]);

  SCHED();
  wt[0] = lds_tr2(apl[0] + 30720, aph[0] + 30720);

  SCHED();
  accb[1] = mfma(wt[1], xf[11], accb[1]);

  SCHED();
  TIE(accb[0]); TIE(accb[1]); TIE(accb[2]);
  cv = *(volatile AS3 unsigned*)(uintptr_t)(M_CNT + 8);

  SCHED();
  wt[1] = lds_tr2(apl[0] + 30752, aph[0] + 30752);

  SCHED();
  accb[2] = mfma(wt[2], xf[11], accb[2]);

  SCHED();
  TIE(accb[0]); TIE(accb[1]); TIE(accb[2]);
  cwait(cv, 3840u, 1024u);

  SCHED();
  wt[2] = lds_tr2(apl[0] + 30784, aph[0] + 30784);

  SCHED();
  accb[0] = mfma(wt[0], xf[12], accb[0]);

  SCHED();
  m0a<49152, 32768>(w6k, kb, kb0); bl<0>(vob, rD, kb);

  wt[0] = lds_tr2(apl[0] + 32256, aph[0] + 32256);

  SCHED();
  accb[1] = mfma(wt[1], xf[12], accb[1]);

  SCHED();
  bl<1024>(vob2, rD, kb);

  wt[1] = lds_tr2(apl[0] + 32288, aph[0] + 32288);

  SCHED();
  accb[2] = mfma(wt[2], xf[12], accb[2]);

  SCHED();
  m0a<51200, 49152>(w6k, kb, kb0); bl<0>(vob, rD, kb);

  wt[2] = lds_tr2(apl[0] + 32320, aph[0] + 32320);

  SCHED();
  accb[0] = mfma(wt[0], xf[13], accb[0]);

  SCHED();
  bl<1024>(vob2, rD, kb);

  wt[0] = lds_tr2(apl[0] + 33792, aph[0] + 33792);

  SCHED();
  accb[1] = mfma(wt[1], xf[13], accb[1]);

  SCHED();
  TIES(c.rE); kb0 = ((1 + c.rE) & 3) * 458752 + kE; m0a<53248, 0>(w6k, kb, kb0); bl<0>(vob, rD, kb);
  TIE(accb[0]); TIE(accb[1]); TIE(accb[2]);
  waitv<5>();

  set_flag(c, flg + 0);

  SCHED();
  wt[1] = lds_tr2(apl[0] + 33824, aph[0] + 33824);

  SCHED();
  accb[2] = mfma(wt[2], xf[13], accb[2]);

  SCHED();
  bl<1024>(vob2, rD, kb);

  wt[2] = lds_tr2(apl[0] + 33856, aph[0] + 33856);

  SCHED();
  accb[0] = mfma(wt[0], xf[14], accb[0]);

  SCHED();
  wt[0] = lds_tr2(apl[0] + 35328, aph[0] + 35328);

  SCHED();
  accb[1] = mfma(wt[1], xf[14], accb[1]);

  SCHED();
  wt[1] = lds_tr2(apl[0] + 35360, aph[0] + 35360);

  SCHED();
  accb[2] = mfma(wt[2], xf[14], accb[2]);

  SCHED();
  TIE(accb[0]); TIE(accb[1]); TIE(accb[2]);
  cv = *(volatile AS3 unsigned*)(uintptr_t)(M_CNT + 8);

  SCHED();
  wt[2] = lds_tr2(apl[0] + 35392, aph[0] + 35392);

  SCHED();
  accb[0] = mfma(wt[0], xf[15], accb[0]);

  SCHED();
  TIE(accb[0]); TIE(accb[1]); TIE(accb[2]);
  cwait(cv, 15u, 8u);

  SCHED();
  csig<4096, 8>();

  SCHED();
  wt[0] = lds_tr2(apl[1] + 0, aph[1] + 0);

  SCHED();
  accb[1] = mfma(wt[1], xf[15], accb[1]);

  SCHED();
  wt[1] = lds_tr2(apl[1] + 32, aph[1] + 32);

  SCHED();
  accb[2] = mfma(wt[2], xf[15], accb[2]);

  SCHED();
  TS(5);
  wt[2] = lds_tr2(apl[1] + 64, aph[1] + 64);

  SCHED();
  accb[0] = mfma(wt[0], xf[16], accb[0]);

  SCHED();
  wt[0] = lds_tr2(apl[1] + 1536, aph[1] + 1536);

  SCHED();
  accb[1] = mfma(wt[1], xf[16], accb[1]);

  SCHED();
  wt[1] = lds_tr2(apl[1] + 1568, aph[1] + 1568);

  SCHED();
  accb[2] = mfma(wt[2], xf[16], accb[2]);

  SCHED();
  wt[2] = lds_tr2(apl[1] + 1600, aph[1] + 1600);

  SCHED();
  accb[0] = mfma(wt[0], xf[17], accb[0]);

  SCHED();
  wt[0] = lds_tr2(apl[1] + 3072, aph[1] + 3072);

  SCHED();
  accb[1] = mfma(wt[1], xf[17], accb[1]);

  SCHED();
  wt[1] = lds_tr2(apl[1] + 3104, aph[1] + 3104);

  SCHED();
  accb[2] = mfma(wt[2], xf[17], accb[2]);

  SCHED();
  wt[2] = lds_tr2(apl[1] + 3136, aph[1] + 3136);

  SCHED();
  accb[0] = mfma(wt[0], xf[18], accb[0]);

  SCHED();
  TIE(accb[0]); TIE(accb[1]); TIE(accb[2]);
  waitv<14>(); csig<16, 8>();

  SCHED();
  wt[0] = lds_tr2(apl[1] + 4608, aph[1] + 4608);

  SCHED();
  accb[1] = mfma(wt[1], xf[18], accb[1]);

  SCHED();
  wt[1] = lds_tr2(apl[1] + 4640, aph[1] + 4640);

  SCHED();
  accb[2] = mfma(wt[2], xf[18], accb[2]);

  SCHED();
  wt[2] = lds_tr2(apl[1] + 4672, aph[1] + 4672);

  SCHED();
  accb[0] = mfma(wt[0], xf[19], accb[0]);

  SCHED();
  wt[0] = lds_tr2(apl[1] + 6144, aph[1] + 6144);

  SCHED();
  accb[1] = mfma(wt[1], xf[19], accb[1]);

  SCHED();
  TIE(accb[0]); TIE(accb[1]); TIE(accb[2]);
  cv = *(volatile AS3 unsigned*)(uintptr_t)(M_CNT + 8);

  SCHED();
  wt[1] = lds_tr2(apl[1] + 6176, aph[1] + 6176);

  SCHED();
  accb[2] = mfma(wt[2], xf[19], accb[2]);

  SCHED();
  TIE(accb[0]); TIE(accb[1]); TIE(accb[2]);
  cwait(cv, 61440u, 16384u);

  SCHED();
  wt[2] = lds_tr2(apl[1] + 6208, aph[1] + 6208);

  SCHED();
  accb[0] = mfma(wt[0], xf[20], accb[0]);

  SCHED();
  m0a<73728, 16384>(w6k, kb, kb0); bl<0>(vob, rD, kb);

  wt[0] = lds_tr2(apl[1] + 7680, aph[1] + 7680);

  SCHED();
  accb[1] = mfma(wt[1], xf[20], accb[1]);

  SCHED();
  bl<1024>(vob2, rD, kb);

  wt[1] = lds_tr2(apl[1] + 7712, aph[1] + 7712);

  SCHED();
  accb[2] = mfma(wt[2], xf[20], accb[2]);

  SCHED();
  m0a<75776, 32768>(w6k, kb, kb0); bl<0>(vob, rD, kb);

  wt[2] = lds_tr2(apl[1] + 7744, aph[1] + 7744);

  SCHED();
  accb[0] = mfma(wt[0], xf[21], accb[0]);

  SCHED();
  bl<1024>(vob2, rD, kb);

  wt[0] = lds_tr2(apl[1] + 9216, aph[1] + 9216);

  SCHED();
  accb[1] = mfma(wt[1], xf[21], accb[1]);

  SCHED();
  m0a<77824, 49152>(w6k, kb, kb0); bl<0>(vob, rD, kb);

  wt[1] = lds_tr2(apl[1] + 9248, aph[1] + 9248);

  SCHED();
  accb[2] = mfma(wt[2], xf[21], accb[2]);

  SCHED();
  bl<1024>(vob2, rD, kb);

  wt[2] = lds_tr2(apl[1] + 9280, aph[1] + 9280);

  SCHED();
  accb[0] = mfma(wt[0], xf[22], accb[0]);

  SCHED();
  wt[0] = lds_tr2(apl[1] + 10752, aph[1] + 10752);

  SCHED();
  accb[1] = mfma(wt[1], xf[22], accb[1]);

  SCHED();
  wt[1] = lds_tr2(apl[1] + 10784, aph[1] + 10784);

  SCHED();
  accb[2] = mfma(wt[2], xf[22], accb[2]);

  SCHED();
  TIE(accb[0]); TIE(accb[1]); TIE(accb[2]);
  cv = *(volatile AS3 unsigned*)(uintptr_t)(M_CNT + 8);

  SCHED();
  wt[2] = lds_tr2(apl[1] + 10816, aph[1] + 10816);

  SCHED();
  accb[0] = mfma(wt[0], xf[23], accb[0]);

  SCHED();
  TIE(accb[0]); TIE(accb[1]); TIE(accb[2]);
  cwait(cv, 240u, 128u);

  SCHED();
  csig<65536, 8>();

  SCHED();
  wt[0] = lds_tr2(apl[1] + 24576, aph[1] + 24576);

  SCHED();
  accb[1] = mfma(wt[1], xf[23], accb[1]);

  SCHED();
  wt[1] = lds_tr2(apl[1] + 26112, aph[1] + 26112);

  SCHED();
  accb[2] = mfma(wt[2], xf[23], accb[2]);

  SCHED();
  TS(6);
  wt[2] = lds_tr2(apl[1] + 27648, aph[1] + 27648);

  SCHED();
  accb[0] = mfma(wt[0], xf[24], accb[0]);

  SCHED();
  wt[0] = lds_tr2(apl[1] + 29184, aph[1] + 29184);

  SCHED();
  accb[0] = mfma(wt[1], xf[25], accb[0]);

  SCHED();
  wt[1] = lds_tr2(apl[1] + 30720, aph[1] + 30720);

  SCHED();
  accb[0] = mfma(wt[2], xf[26], accb[0]);

  SCHED();
  wt[2] = lds_tr2(apl[1] + 32256, aph[1] + 32256);

  SCHED();
  accb[0] = mfma(wt[0], xf[27], accb[0]);

  SCHED();
  wt[0] = lds_tr2(apl[1] + 33792, aph[1] + 33792);

  SCHED();
  accb[0] = mfma(wt[1], xf[28], accb[0]);

  SCHED();
  wt[1] = lds_tr2(apl[1] + 35328, aph[1] + 35328);

  SCHED();
  accb[0] = mfma(wt[2], xf[29], accb[0]);

  SCHED();
  wt[2] = lds_tr2(apl[1] + 24608, aph[1] + 24608);

  SCHED();
  accb[0] = mfma(wt[0], xf[30], accb[0]);

  SCHED();
  wt[0] = lds_tr2(apl[1] + 26144, aph[1] + 26144);

  SCHED();
  accb[0] = mfma(wt[1], xf[31], accb[0]);

  SCHED();
  wt[1] = lds_tr2(apl[1] + 27680, aph[1] + 27680);

  SCHED();
  accb[1] = mfma(wt[2], xf[24], accb[1]);

  SCHED();
  wt[2] = lds_tr2(apl[1] + 29216, aph[1] + 29216);

  SCHED();
  accb[1] = mfma(wt[0], xf[25], accb[1]);

  SCHED();
  TIE(accb[1]);
  Glu q00;

  glu_st<0>(accb[0], q00, nr, ir);

  SCHED();
  wt[0] = lds_tr2(apl[1] + 30752, aph[1] + 30752);

  SCHED();
  accb[1] = mfma(wt[1], xf[26], accb[1]);

  SCHED();
  TIE(accb[1]);
  cv = *(volatile AS3 unsigned*)(uintptr_t)(M_CNT + 8);

  SCHED();
  glu_st<1>(accb[0], q00, nr, ir);

  SCHED();
  wt[1] = lds_tr2(apl[1] + 32288, aph[1] + 32288);

  SCHED();
  accb[1] = mfma(wt[2], xf[27], accb[1]);

  SCHED();
  TIE(accb[1]);
  cwait(cv, 983040u, 262144u);

  SCHED();
  wt[2] = lds_tr2(apl[1] + 33824, aph[1] + 33824);

  SCHED();
  accb[1] = mfma(wt[0], xf[28], accb[1]);

  SCHED();
  TIES(c.rE); kb0 = ((2 + c.rE) & 3) * 458752 + kE; m0a<98304, 0>(w6k, kb, kb0); bl<0>(vob, rD, kb);
  TIE(accb[1]);
  glu_st<2>(accb[0], q00, nr, ir);

  SCHED();
  wt[0] = lds_tr2(apl[1] + 35360, aph[1] + 35360);

  SCHED();
  accb[1] = mfma(wt[1], xf[29], accb[1]);

  SCHED();
  bl<1024>(vob2, rD, kb);

  TIE(accb[1]);
  glu_st<3>(accb[0], q00, nr, ir);

  SCHED();
  wt[1] = lds_tr2(apl[1] + 24640, aph[1] + 24640);

  SCHED();
  accb[1] = mfma(wt[2], xf[30], accb[1]);

  SCHED();
  m0a<100352, 16384>(w6k, kb, kb0); bl<0>(vob, rD, kb);

  wt[2] = lds_tr2(apl[1] + 26176, aph[1] + 26176);

  SCHED();
  accb[1] = mfma(wt[0], xf[31], accb[1]);

  SCHED();
  bl<1024>(vob2, rD, kb);

  TIE(accb[1]);
  glu_st<4>(accb[0], q00, nr, ir);

  SCHED();
  wt[0] = lds_tr2(apl[1] + 27712, aph[1] + 27712);

  SCHED();
  accb[2] = mfma(wt[1], xf[24], accb[2]);

  SCHED();
  m0a<102400, 32768>(w6k, kb, kb0); bl<0>(vob, rD, kb);

  TIE(accb[2]);
  glu_st<5>(accb[0], q00, nr, ir);

  gst16s(l16, q00.o, hgb + (c.u0 + 0) * 1024);

  SCHED();
  wt[1] = lds_tr2(apl[1] + 29248, aph[1] + 29248);

  SCHED();
  accb[2] = mfma(wt[2], xf[25], accb[2]);

  SCHED();
  bl<1024>(vob2, rD, kb);

  TIE(accb[2]);
  Glu q01;

  glu_st<0>(accb[1], q01, nr, ir);

  SCHED();
  wt[2] = lds_tr2(apl[1] + 30784, aph[1] + 30784);

  SCHED();
  accb[2] = mfma(wt[0], xf[26], accb[2]);

  SCHED();
  TIE(accb[2]);
  glu_st<1>(accb[1], q01, nr, ir);

  SCHED();
  { int l2 = c.lane; TIE(l2);
    fa = W_FLG + (unsigned)(mtp * 4096 + wmp * 8 + wm * 256 + (((l2 >> 2) + c.rE) & 3) * 1024 + (l2 & 3) * 32);
    va1 = l16 + 112 * 1024; }

  gld16s(f, fa, c.ws);

  SCHED();
  wt[0] = lds_tr2(apl[1] + 32320, aph[1] + 32320);

  SCHED();
  accb[2] = mfma(wt[1], xf[27], accb[2]);

  SCHED();
  TIE(accb[2]);
  glu_st<2>(accb[1], q01, nr, ir);

  SCHED();
  wt[1] = lds_tr2(apl[1] + 33856, aph[1] + 33856);

  SCHED();
  accb[2] = mfma(wt[2], xf[28], accb[2]);

  SCHED();
  TIE(accb[2]);
  glu_st<3>(accb[1], q01, nr, ir);

  SCHED();
  wt[2] = lds_tr2(apl[1] + 35392, aph[1] + 35392);

  SCHED();
  accb[2] = mfma(wt[0], xf[29], accb[2]);

  SCHED();
  TIE(accb[2]);
  csig<256, 4>();

  SCHED();
  glu_st<4>(accb[1], q01, nr, ir);

  SCHED();
  SCHED();
  accb[2] = mfma(wt[1], xf[30], accb[2]);

  SCHED();
  TIE(accb[2]);
  glu_st<5>(accb[1], q01, nr, ir);

  gst16s(l16, q01.o, hgb + (c.u0 + 1) * 1024);

  SCHED();
  SCHED();
  accb[2] = mfma(wt[2], xf[31], accb[2]);

  SCHED();
  TIE(accb[2]);
  TS(7);
  asm volatile("s_nop 7\n\ts_nop 3");

  Glu q02;

  glu_st<0>(accb[2], q02, nr, ir);

  glu_st<1>(accb[2], q02, nr, ir);

  glu_st<2>(accb[2], q02, nr, ir);

  glu_st<3>(accb[2], q02, nr, ir);

  glu_st<4>(accb[2], q02, nr, ir);

  glu_st<5>(accb[2], q02, nr, ir);

  gst16s(l16, q02.o, hgb + (c.u0 + 2) * 1024);

  asm volatile("s_waitcnt vmcnt(2)" : "+v"(f)::"memory");

  unsigned ready = flags_ok(c, f);

  bool lset = false;

  if ((ready & 3u) != 3u && !lset) { waitv<0>(); set_flag(c, flg + 64); lset = true; }

  wait_flags(c, fa, ready, 3);

  TIES(c.rE); const char* pa0 = haE + ((0 + c.rE) & 3) * 28672; gld16so<0>(A[0][0], l16, pa0);
  gld16so<0>(A[0][1], va1, pa0);

  gld16so<1024>(A[1][0], l16, pa0);

  gld16so<1024>(A[1][1], va1, pa0);

  gld16so<2048>(A[2][0], l16, pa0);

  gld16so<2048>(A[2][1], va1, pa0);

  gld16so<3072>(A[3][0], l16, pa0);

  gld16so<3072>(A[3][1], va1, pa0);

  if (!lset) { waitv<8>(); set_flag(c, flg + 64); }

  TS(8);
  wait_flags(c, fa, ready, 48);

  TIES(c.rE); const char* pa4 = haE + ((1 + c.rE) & 3) * 28672; gld16so<0>(A[4][0], l16, pa4);
  gld16so<0>(A[4][1], va1, pa4);

  gld16so<1024>(A[5][0], l16, pa4);

  gld16so<1024>(A[5][1], va1, pa4);

  gld16so<2048>(A[6][0], l16, pa4);

  gld16so<2048>(A[6][1], va1, pa4);

  gld16so<3072>(A[7][0], l16, pa4);

  gld16so<3072>(A[7][1], va1, pa4);

  wait_flags(c, fa, ready, 768);

  TIES(c.rE); const char* pa8 = haE + ((2 + c.rE) & 3) * 28672; gld16so<0>(A[8][0], l16, pa8);
  gld16so<0>(A[8][1], va1, pa8);

  gld16so<1024>(A[9][0], l16, pa8);

  gld16so<1024>(A[9][1], va1, pa8);

  gld16so<2048>(A[10][0], l16, pa8);

  gld16so<2048>(A[10][1], va1, pa8);

  gld16so<3072>(A[11][0], l16, pa8);

  gld16so<3072>(A[11][1], va1, pa8);

  wait_flags(c, fa, ready, 12288);

  TIES(c.rE); const char* pa12 = haE + ((3 + c.rE) & 3) * 28672; gld16so<0>(A[12][0], l16, pa12);
  gld16so<0>(A[12][1], va1, pa12);

  gld16so<1024>(A[13][0], l16, pa12);

  gld16so<1024>(A[13][1], va1, pa12);

  gld16so<2048>(A[14][0], l16, pa12);

  gld16so<2048>(A[14][1], va1, pa12);

  gld16so<3072>(A[15][0], l16, pa12);

  gld16so<3072>(A[15][1], va1, pa12);

  TS(13);
  {
    int l2 = c.lane; TIE(l2);
    int g16 = l2 >> 4, cb = g16 & 1, hh = g16 >> 1, q4 = (l2 >> 2) & 3, p4 = l2 & 3;
    unsigned b = (unsigned)((4 * hh + q4) * 128 + cb * 32 + p4 * 8) + (unsigned)(((q4 >> 1) & 1) * 64);
    ab[0][0] = b + w4k + 32768; ab[0][1] = ab[0][0] ^ 64u; ab[1][0] = b + w6k + 49152; ab[1][1] = ab[1][0] ^ 64u;
    TIE(ab[0][0]); TIE(ab[0][1]); TIE(ab[1][0]); TIE(ab[1][1]);
  }

  f32x16 acc2[2][2];
#pragma unroll
  for (int a = 0; a < 2; a++)
#pragma unroll
    for (int b = 0; b < 2; b++)
#pragma unroll
      for (int i = 0; i < 16; i++) acc2[a][b][i] = 0.f;

  asm volatile("s_waitcnt vmcnt(28)" : "+v"(A[0][0]), "+v"(A[0][1]), "+v"(A[1][0]), "+v"(A[1][1])::"memory");

  {
    u32x4 b00 = lds_tr2(ab[0][0] + 0, ab[0][0] + 1024), b10 = lds_tr2(ab[0][1] + 0, ab[0][1] + 1024);

    u32x4 b01 = lds_tr2(ab[0][0] + 2048, ab[0][0] + 3072), b11 = lds_tr2(ab[0][1] + 2048, ab[0][1] + 3072);

    acc2[0][0] = mfma(b00, A[0][0], acc2[0][0]); acc2[0][1] = mfma(b10, A[0][0], acc2[0][1]);

    acc2[1][0] = mfma(b00, A[0][1], acc2[1][0]); acc2[1][1] = mfma(b10, A[0][1], acc2[1][1]);

    acc2[0][0] = mfma(b01, A[1][0], acc2[0][0]); acc2[0][1] = mfma(b11, A[1][0], acc2[0][1]);

    acc2[1][0] = mfma(b01, A[1][1], acc2[1][0]); acc2[1][1] = mfma(b11, A[1][1], acc2[1][1]);

  }

  TIE(acc2[0][0]); TIE(acc2[0][1]); TIE(acc2[1][0]); TIE(acc2[1][1]);
  waitl();

  m0a<32768, 49152>(w4k, kb, kb0); bl<0>(vob, rD, kb);

  bl<1024>(vob2, rD, kb);

  TIES(c.rE); kb0 = ((3 + c.rE) & 3) * 458752 + kE; m0a<34816, 0>(w4k, kb, kb0); bl<0>(vob, rD, kb);
  bl<1024>(vob2, rD, kb);

  TS(14);
  asm volatile("s_waitcnt vmcnt(28)" : "+v"(A[2][0]), "+v"(A[2][1]), "+v"(A[3][0]), "+v"(A[3][1])::"memory");

  {
    u32x4 b00 = lds_tr2(ab[1][0] + 0, ab[1][0] + 1024), b10 = lds_tr2(ab[1][1] + 0, ab[1][1] + 1024);

    u32x4 b01 = lds_tr2(ab[1][0] + 2048, ab[1][0] + 3072), b11 = lds_tr2(ab[1][1] + 2048, ab[1][1] + 3072);

    acc2[0][0] = mfma(b00, A[2][0], acc2[0][0]); acc2[0][1] = mfma(b10, A[2][0], acc2[0][1]);

    acc2[1][0] = mfma(b00, A[2][1], acc2[1][0]); acc2[1][1] = mfma(b10, A[2][1], acc2[1][1]);

    acc2[0][0] = mfma(b01, A[3][0], acc2[0][0]); acc2[0][1] = mfma(b11, A[3][0], acc2[0][1]);

    acc2[1][0] = mfma(b01, A[3][1], acc2[1][0]); acc2[1][1] = mfma(b11, A[3][1], acc2[1][1]);

  }

  TIE(acc2[0][0]); TIE(acc2[0][1]); TIE(acc2[1][0]); TIE(acc2[1][1]);
  waitl();

  m0a<49152, 16384>(w6k, kb, kb0); bl<0>(vob, rD, kb);

  bl<1024>(vob2, rD, kb);

  m0a<51200, 32768>(w6k, kb, kb0); bl<0>(vob, rD, kb);

  bl<1024>(vob2, rD, kb);

  gld16s(f, fa, c.ws);

  asm volatile("s_waitcnt vmcnt(29)" : "+v"(A[4][0]), "+v"(A[4][1]), "+v"(A[5][0]), "+v"(A[5][1])::"memory");

  {
    u32x4 b00 = lds_tr2(ab[1][0] + 4096, ab[1][0] + 5120), b10 = lds_tr2(ab[1][1] + 4096, ab[1][1] + 5120);

    u32x4 b01 = lds_tr2(ab[1][0] + 24576, ab[1][0] + 25600), b11 = lds_tr2(ab[1][1] + 24576, ab[1][1] + 25600);

    acc2[0][0] = mfma(b00, A[4][0], acc2[0][0]); acc2[0][1] = mfma(b10, A[4][0], acc2[0][1]);

    acc2[1][0] = mfma(b00, A[4][1], acc2[1][0]); acc2[1][1] = mfma(b10, A[4][1], acc2[1][1]);

    acc2[0][0] = mfma(b01, A[5][0], acc2[0][0]); acc2[0][1] = mfma(b11, A[5][0], acc2[0][1]);

    acc2[1][0] = mfma(b01, A[5][1], acc2[1][0]); acc2[1][1] = mfma(b11, A[5][1], acc2[1][1]);

  }

  TIE(acc2[0][0]); TIE(acc2[0][1]); TIE(acc2[1][0]); TIE(acc2[1][1]);
  waitl();

  m0a<53248, 49152>(w6k, kb, kb0); bl<0>(vob, rD, kb);

  bl<1024>(vob2, rD, kb);

  TIES(c.rE); kb0 = ((0 + c.rE) & 3) * 458752 + kL; m0a<73728, 0>(w6k, kb, kb0); bl<0>(vob, rD, kb);
  bl<1024>(vob2, rD, kb);

  asm volatile("s_waitcnt vmcnt(29)" : "+v"(A[6][0]), "+v"(A[6][1]), "+v"(A[7][0]), "+v"(A[7][1])::"memory");

  {
    u32x4 b00 = lds_tr2(ab[1][0] + 26624, ab[1][0] + 27648), b10 = lds_tr2(ab[1][1] + 26624, ab[1][1] + 27648);

    u32x4 b01 = lds_tr2(ab[1][0] + 28672, ab[1][0] + 29696), b11 = lds_tr2(ab[1][1] + 28672, ab[1][1] + 29696);

    acc2[0][0] = mfma(b00, A[6][0], acc2[0][0]); acc2[0][1] = mfma(b10, A[6][0], acc2[0][1]);

    acc2[1][0] = mfma(b00, A[6][1], acc2[1][0]); acc2[1][1] = mfma(b10, A[6][1], acc2[1][1]);

    acc2[0][0] = mfma(b01, A[7][0], acc2[0][0]); acc2[0][1] = mfma(b11, A[7][0], acc2[0][1]);

    acc2[1][0] = mfma(b01, A[7][1], acc2[1][0]); acc2[1][1] = mfma(b11, A[7][1], acc2[1][1]);

  }

  TIE(acc2[0][0]); TIE(acc2[0][1]); TIE(acc2[1][0]); TIE(acc2[1][1]);
  waitl();

  m0a<75776, 16384>(w6k, kb, kb0); bl<0>(vob, rD, kb);

  bl<1024>(vob2, rD, kb);

  m0a<77824, 32768>(w6k, kb, kb0); bl<0>(vob, rD, kb);

  bl<1024>(vob2, rD, kb);

  TS(9);
  asm volatile("s_waitcnt vmcnt(8)" : "+v"(f)::"memory");

  ready = flags_ok(c, f);

  wait_flags(c, fa, ready, 0x4444);

  TIES(c.rE); const char* pa16 = haL + ((0 + c.rE) & 3) * 28672; gld16so<0>(A[16][0], l16, pa16);
  gld16so<0>(A[16][1], va1, pa16);

  gld16so<1024>(A[17][0], l16, pa16);

  gld16so<1024>(A[17][1], va1, pa16);

  gld16so<2048>(A[18][0], l16, pa16);

  gld16so<2048>(A[18][1], va1, pa16);

  TIES(c.rE); const char* pa19 = haL + ((1 + c.rE) & 3) * 28672; gld16so<0>(A[19][0], l16, pa19);
  gld16so<0>(A[19][1], va1, pa19);

  gld16so<1024>(A[20][0], l16, pa19);

  gld16so<1024>(A[20][1], va1, pa19);

  gld16so<2048>(A[21][0], l16, pa19);

  gld16so<2048>(A[21][1], va1, pa19);

  TIES(c.rE); const char* pa22 = haL + ((2 + c.rE) & 3) * 28672; gld16so<0>(A[22][0], l16, pa22);
  gld16so<0>(A[22][1], va1, pa22);

  gld16so<1024>(A[23][0], l16, pa22);

  gld16so<1024>(A[23][1], va1, pa22);

  gld16so<2048>(A[24][0], l16, pa22);

  gld16so<2048>(A[24][1], va1, pa22);

  TIES(c.rE); const char* pa25 = haL + ((3 + c.rE) & 3) * 28672; gld16so<0>(A[25][0], l16, pa25);
  gld16so<0>(A[25][1], va1, pa25);

  gld16so<1024>(A[26][0], l16, pa25);

  gld16so<1024>(A[26][1], va1, pa25);

  gld16so<2048>(A[27][0], l16, pa25);

  gld16so<2048>(A[27][1], va1, pa25);

  TS(10);
  asm volatile("s_waitcnt vmcnt(53)" : "+v"(A[8][0]), "+v"(A[8][1]), "+v"(A[9][0]), "+v"(A[9][1])::"memory");

  {
    u32x4 b00 = lds_tr2(ab[1][0] + 49152, ab[1][0] + 50176), b10 = lds_tr2(ab[1][1] + 49152, ab[1][1] + 50176);

    u32x4 b01 = lds_tr2(ab[1][0] + 51200, ab[1][0] + 52224), b11 = lds_tr2(ab[1][1] + 51200, ab[1][1] + 52224);

    acc2[0][0] = mfma(b00, A[8][0], acc2[0][0]); acc2[0][1] = mfma(b10, A[8][0], acc2[0][1]);

    acc2[1][0] = mfma(b00, A[8][1], acc2[1][0]); acc2[1][1] = mfma(b10, A[8][1], acc2[1][1]);

    acc2[0][0] = mfma(b01, A[9][0], acc2[0][0]); acc2[0][1] = mfma(b11, A[9][0], acc2[0][1]);

    acc2[1][0] = mfma(b01, A[9][1], acc2[1][0]); acc2[1][1] = mfma(b11, A[9][1], acc2[1][1]);

  }

  TIE(acc2[0][0]); TIE(acc2[0][1]); TIE(acc2[1][0]); TIE(acc2[1][1]);
  waitl();

  TIES(c.rE); kb0 = ((1 + c.rE) & 3) * 458752 + kL; m0a<98304, 0>(w6k, kb, kb0); bl<0>(vob, rD, kb);
  bl<1024>(vob2, rD, kb);

  m0a<100352, 16384>(w6k, kb, kb0); bl<0>(vob, rD, kb);

  bl<1024>(vob2, rD, kb);

  asm volatile("s_waitcnt vmcnt(43)" : "+v"(A[10][0]), "+v"(A[10][1]), "+v"(A[11][0]), "+v"(A[11][1])::"memory");

  {
    u32x4 b00 = lds_tr2(ab[1][0] + 53248, ab[1][0] + 54272), b10 = lds_tr2(ab[1][1] + 53248, ab[1][1] + 54272);

    u32x4 b01 = lds_tr2(ab[0][0] + 0, ab[0][0] + 1024), b11 = lds_tr2(ab[0][1] + 0, ab[0][1] + 1024);

    acc2[0][0] = mfma(b00, A[10][0], acc2[0][0]); acc2[0][1] = mfma(b10, A[10][0], acc2[0][1]);

    acc2[1][0] = mfma(b00, A[10][1], acc2[1][0]); acc2[1][1] = mfma(b10, A[10][1], acc2[1][1]);

    acc2[0][0] = mfma(b01, A[11][0], acc2[0][0]); acc2[0][1] = mfma(b11, A[11][0], acc2[0][1]);

    acc2[1][0] = mfma(b01, A[11][1], acc2[1][0]); acc2[1][1] = mfma(b11, A[11][1], acc2[1][1]);

  }

  TIE(acc2[0][0]); TIE(acc2[0][1]); TIE(acc2[1][0]); TIE(acc2[1][1]);
  waitl();

  m0a<102400, 32768>(w6k, kb, kb0); bl<0>(vob, rD, kb);

  bl<1024>(vob2, rD, kb);

  TIES(c.rE); kb0 = ((2 + c.rE) & 3) * 458752 + kL; m0a<32768, 0>(w4k, kb, kb0); bl<0>(vob, rD, kb);
  bl<1024>(vob2, rD, kb);

  asm volatile("s_waitcnt vmcnt(43)" : "+v"(A[12][0]), "+v"(A[12][1]), "+v"(A[13][0]), "+v"(A[13][1])::"memory");

  {
    u32x4 b00 = lds_tr2(ab[0][0] + 2048, ab[0][0] + 3072), b10 = lds_tr2(ab[0][1] + 2048, ab[0][1] + 3072);

    u32x4 b01 = lds_tr2(ab[1][0] + 0, ab[1][0] + 1024), b11 = lds_tr2(ab[1][1] + 0, ab[1][1] + 1024);

    acc2[0][0] = mfma(b00, A[12][0], acc2[0][0]); acc2[0][1] = mfma(b10, A[12][0], acc2[0][1]);

    acc2[1][0] = mfma(b00, A[12][1], acc2[1][0]); acc2[1][1] = mfma(b10, A[12][1], acc2[1][1]);

    acc2[0][0] = mfma(b01, A[13][0], acc2[0][0]); acc2[0][1] = mfma(b11, A[13][0], acc2[0][1]);

    acc2[1][0] = mfma(b01, A[13][1], acc2[1][0]); acc2[1][1] = mfma(b11, A[13][1], acc2[1][1]);

  }

  TIE(acc2[0][0]); TIE(acc2[0][1]); TIE(acc2[1][0]); TIE(acc2[1][1]);
  waitl();

  m0a<34816, 16384>(w4k, kb, kb0); bl<0>(vob, rD, kb);

  bl<1024>(vob2, rD, kb);

  m0a<49152, 32768>(w6k, kb, kb0); bl<0>(vob, rD, kb);

  bl<1024>(vob2, rD, kb);

  asm volatile("s_waitcnt vmcnt(42)" : "+v"(A[14][0]), "+v"(A[14][1]), "+v"(A[15][0]), "+v"(A[15][1])::"memory");

  {
    u32x4 b00 = lds_tr2(ab[1][0] + 2048, ab[1][0] + 3072), b10 = lds_tr2(ab[1][1] + 2048, ab[1][1] + 3072);

    u32x4 b01 = lds_tr2(ab[1][0] + 4096, ab[1][0] + 5120), b11 = lds_tr2(ab[1][1] + 4096, ab[1][1] + 5120);

    acc2[0][0] = mfma(b00, A[14][0], acc2[0][0]); acc2[0][1] = mfma(b10, A[14][0], acc2[0][1]);

    acc2[1][0] = mfma(b00, A[14][1], acc2[1][0]); acc2[1][1] = mfma(b10, A[14][1], acc2[1][1]);

    acc2[0][0] = mfma(b01, A[15][0], acc2[0][0]); acc2[0][1] = mfma(b11, A[15][0], acc2[0][1]);

    acc2[1][0] = mfma(b01, A[15][1], acc2[1][0]); acc2[1][1] = mfma(b11, A[15][1], acc2[1][1]);

  }

  TIE(acc2[0][0]); TIE(acc2[0][1]); TIE(acc2[1][0]); TIE(acc2[1][1]);
  waitl();

  TIES(c.rE); kb0 = ((3 + c.rE) & 3) * 458752 + kL; m0a<51200, 0>(w6k, kb, kb0); bl<0>(vob, rD, kb);
  bl<1024>(vob2, rD, kb);

  m0a<53248, 16384>(w6k, kb, kb0); bl<0>(vob, rD, kb);

  bl<1024>(vob2, rD, kb);

  asm volatile("s_waitcnt vmcnt(34)" : "+v"(A[16][0]), "+v"(A[16][1]), "+v"(A[17][0]), "+v"(A[17][1]), "+v"(A[18][0]), "+v"(A[18][1])::"memory");

  {
    u32x4 b00 = lds_tr2(ab[1][0] + 24576, ab[1][0] + 25600), b10 = lds_tr2(ab[1][1] + 24576, ab[1][1] + 25600);

    u32x4 b01 = lds_tr2(ab[1][0] + 26624, ab[1][0] + 27648), b11 = lds_tr2(ab[1][1] + 26624, ab[1][1] + 27648);

    u32x4 b02 = lds_tr2(ab[1][0] + 28672, ab[1][0] + 29696), b12 = lds_tr2(ab[1][1] + 28672, ab[1][1] + 29696);

    acc2[0][0] = mfma(b00, A[16][0], acc2[0][0]); acc2[0][1] = mfma(b10, A[16][0], acc2[0][1]);

    acc2[1][0] = mfma(b00, A[16][1], acc2[1][0]); acc2[1][1] = mfma(b10, A[16][1], acc2[1][1]);

    acc2[0][0] = mfma(b01, A[17][0], acc2[0][0]); acc2[0][1] = mfma(b11, A[17][0], acc2[0][1]);

    acc2[1][0] = mfma(b01, A[17][1], acc2[1][0]); acc2[1][1] = mfma(b11, A[17][1], acc2[1][1]);

    acc2[0][0] = mfma(b02, A[18][0], acc2[0][0]); acc2[0][1] = mfma(b12, A[18][0], acc2[0][1]);

    acc2[1][0] = mfma(b02, A[18][1], acc2[1][0]); acc2[1][1] = mfma(b12, A[18][1], acc2[1][1]);

  }

  TIE(acc2[0][0]); TIE(acc2[0][1]); TIE(acc2[1][0]); TIE(acc2[1][1]);
  waitl();

  m0a<73728, 32768>(w6k, kb, kb0); bl<0>(vob, rD, kb);

  bl<1024>(vob2, rD, kb);

  asm volatile("s_waitcnt vmcnt(12)" : "+v"(A[19][0]), "+v"(A[19][1]), "+v"(A[20][0]), "+v"(A[20][1]), "+v"(A[21][0]), "+v"(A[21][1])::"memory");

  {
    u32x4 b00 = lds_tr2(ab[1][0] + 49152, ab[1][0] + 50176), b10 = lds_tr2(ab[1][1] + 49152, ab[1][1] + 50176);

    u32x4 b01 = lds_tr2(ab[1][0] + 51200, ab[1][0] + 52224), b11 = lds_tr2(ab[1][1] + 51200, ab[1][1] + 52224);

    u32x4 b02 = lds_tr2(ab[1][0] + 53248, ab[1][0] + 54272), b12 = lds_tr2(ab[1][1] + 53248, ab[1][1] + 54272);

    acc2[0][0] = mfma(b00, A[19][0], acc2[0][0]); acc2[0][1] = mfma(b10, A[19][0], acc2[0][1]);

    acc2[1][0] = mfma(b00, A[19][1], acc2[1][0]); acc2[1][1] = mfma(b10, A[19][1], acc2[1][1]);

    acc2[0][0] = mfma(b01, A[20][0], acc2[0][0]); acc2[0][1] = mfma(b11, A[20][0], acc2[0][1]);

    acc2[1][0] = mfma(b01, A[20][1], acc2[1][0]); acc2[1][1] = mfma(b11, A[20][1], acc2[1][1]);

    acc2[0][0] = mfma(b02, A[21][0], acc2[0][0]); acc2[0][1] = mfma(b12, A[21][0], acc2[0][1]);

    acc2[1][0] = mfma(b02, A[21][1], acc2[1][0]); acc2[1][1] = mfma(b12, A[21][1], acc2[1][1]);

  }

  TIE(acc2[0][0]); TIE(acc2[0][1]); TIE(acc2[1][0]); TIE(acc2[1][1]);
  waitl();

  EPI_LOADS
  asm volatile("s_waitcnt vmcnt(11)" : "+v"(A[22][0]), "+v"(A[22][1]), "+v"(A[23][0]), "+v"(A[23][1]), "+v"(A[24][0]), "+v"(A[24][1])::"memory");

  {
    u32x4 b00 = lds_tr2(ab[0][0] + 0, ab[0][0] + 1024), b10 = lds_tr2(ab[0][1] + 0, ab[0][1] + 1024);

    u32x4 b01 = lds_tr2(ab[0][0] + 2048, ab[0][0] + 3072), b11 = lds_tr2(ab[0][1] + 2048, ab[0][1] + 3072);

    u32x4 b02 = lds_tr2(ab[1][0] + 0, ab[1][0] + 1024), b12 = lds_tr2(ab[1][1] + 0, ab[1][1] + 1024);

    acc2[0][0] = mfma(b00, A[22][0], acc2[0][0]); acc2[0][1] = mfma(b10, A[22][0], acc2[0][1]);

    acc2[1][0] = mfma(b00, A[22][1], acc2[1][0]); acc2[1][1] = mfma(b10, A[22][1], acc2[1][1]);

    acc2[0][0] = mfma(b01, A[23][0], acc2[0][0]); acc2[0][1] = mfma(b11, A[23][0], acc2[0][1]);

    acc2[1][0] = mfma(b01, A[23][1], acc2[1][0]); acc2[1][1] = mfma(b11, A[23][1], acc2[1][1]);

    acc2[0][0] = mfma(b02, A[24][0], acc2[0][0]); acc2[0][1] = mfma(b12, A[24][0], acc2[0][1]);

    acc2[1][0] = mfma(b02, A[24][1], acc2[1][0]); acc2[1][1] = mfma(b12, A[24][1], acc2[1][1]);

  }

  TIE(acc2[0][0]); TIE(acc2[0][1]); TIE(acc2[1][0]); TIE(acc2[1][1]);
  waitl();

  asm volatile("s_waitcnt vmcnt(5)" : "+v"(A[25][0]), "+v"(A[25][1]), "+v"(A[26][0]), "+v"(A[26][1]), "+v"(A[27][0]), "+v"(A[27][1])::"memory");

  {
    u32x4 b00 = lds_tr2(ab[1][0] + 2048, ab[1][0] + 3072), b10 = lds_tr2(ab[1][1] + 2048, ab[1][1] + 3072);

    u32x4 b01 = lds_tr2(ab[1][0] + 4096, ab[1][0] + 5120), b11 = lds_tr2(ab[1][1] + 4096, ab[1][1] + 5120);

    u32x4 b02 = lds_tr2(ab[1][0] + 24576, ab[1][0] + 25600), b12 = lds_tr2(ab[1][1] + 24576, ab[1][1] + 25600);

    acc2[0][0] = mfma(b00, A[25][0], acc2[0][0]); acc2[0][1] = mfma(b10, A[25][0], acc2[0][1]);

    acc2[1][0] = mfma(b00, A[25][1], acc2[1][0]); acc2[1][1] = mfma(b10, A[25][1], acc2[1][1]);

    acc2[0][0] = mfma(b01, A[26][0], acc2[0][0]); acc2[0][1] = mfma(b11, A[26][0], acc2[0][1]);

    acc2[1][0] = mfma(b01, A[26][1], acc2[1][0]); acc2[1][1] = mfma(b11, A[26][1], acc2[1][1]);

    acc2[0][0] = mfma(b02, A[27][0], acc2[0][0]); acc2[0][1] = mfma(b12, A[27][0], acc2[0][1]);

    acc2[1][0] = mfma(b02, A[27][1], acc2[1][0]); acc2[1][1] = mfma(b12, A[27][1], acc2[1][1]);

  }

  TIE(acc2[0][0]); TIE(acc2[0][1]); TIE(acc2[1][0]); TIE(acc2[1][1]);
  waitl();

  TS(11);
  {
    int l2 = c.lane; TIE(l2);
    unsigned bw = (unsigned)(49152 + w6k + (l2 >> 5) * 512 + (((l2 & 31) ^ (l2 >> 5)) << 4));

    { unsigned a = bw ^ 0u; TIE(a);

      { f32x4 v = {acc2[0][0][0], acc2[0][0][1], acc2[0][0][2], acc2[0][0][3]}; lds_wf4(a + 0, v); }

      { f32x4 v = {acc2[1][0][0], acc2[1][0][1], acc2[1][0][2], acc2[1][0][3]}; lds_wf4(a + 26624, v); }

    }

    { unsigned a = bw ^ 32u; TIE(a);

      { f32x4 v = {acc2[0][0][4], acc2[0][0][5], acc2[0][0][6], acc2[0][0][7]}; lds_wf4(a + 1024, v); }

      { f32x4 v = {acc2[1][0][4], acc2[1][0][5], acc2[1][0][6], acc2[1][0][7]}; lds_wf4(a + 27648, v); }

    }

    { unsigned a = bw ^ 64u; TIE(a);

      { f32x4 v = {acc2[0][0][8], acc2[0][0][9], acc2[0][0][10], acc2[0][0][11]}; lds_wf4(a + 2048, v); }

      { f32x4 v = {acc2[1][0][8], acc2[1][0][9], acc2[1][0][10], acc2[1][0][11]}; lds_wf4(a + 28672, v); }

    }

    { unsigned a = bw ^ 96u; TIE(a);

      { f32x4 v = {acc2[0][0][12], acc2[0][0][13], acc2[0][0][14], acc2[0][0][15]}; lds_wf4(a + 3072, v); }

      { f32x4 v = {acc2[1][0][12], acc2[1][0][13], acc2[1][0][14], acc2[1][0][15]}; lds_wf4(a + 29696, v); }

    }

    { unsigned a = bw ^ 128u; TIE(a);

      { f32x4 v = {acc2[0][1][0], acc2[0][1][1], acc2[0][1][2], acc2[0][1][3]}; lds_wf4(a + 4096, v); }

      { f32x4 v = {acc2[1][1][0], acc2[1][1][1], acc2[1][1][2], acc2[1][1][3]}; lds_wf4(a + 49152, v); }

    }

    { unsigned a = bw ^ 160u; TIE(a);

      { f32x4 v = {acc2[0][1][4], acc2[0][1][5], acc2[0][1][6], acc2[0][1][7]}; lds_wf4(a + 5120, v); }

      { f32x4 v = {acc2[1][1][4], acc2[1][1][5], acc2[1][1][6], acc2[1][1][7]}; lds_wf4(a + 50176, v); }

    }

    { unsigned a = bw ^ 192u; TIE(a);

      { f32x4 v = {acc2[0][1][8], acc2[0][1][9], acc2[0][1][10], acc2[0][1][11]}; lds_wf4(a + 24576, v); }

      { f32x4 v = {acc2[1][1][8], acc2[1][1][9], acc2[1][1][10], acc2[1][1][11]}; lds_wf4(a + 51200, v); }

    }

    { unsigned a = bw ^ 224u; TIE(a);

      { f32x4 v = {acc2[0][1][12], acc2[0][1][13], acc2[0][1][14], acc2[0][1][15]}; lds_wf4(a + 25600, v); }

      { f32x4 v = {acc2[1][1][12], acc2[1][1][13], acc2[1][1][14], acc2[1][1][15]}; lds_wf4(a + 52224, v); }

    }

  }

  EPI_WAIT
  }
  }
  {
    int l2 = threadIdx.x & 63;
    TIE(l2);
    const int dq = l2 & 15;
    f32x4 dn[2], y[2];
    const int u = 8 * wn + (dq >> 1), up = (u * 43) >> 8;
    const unsigned da = (unsigned)(WR + up * WSL + (u - 6 * up) * 1024 + (dq & 1) * 512), ya = (unsigned)(dq * 512 + (wn ? SCR : 0));

    bar();
#pragma unroll
    for (int i = 0; i < 2; i++) {
      if (i == 0) {  } if (i == 1) {  }
      unsigned ro = (unsigned)(((8 * wm + 4 * i + (l2 >> 4)) ^ dq) << 4);
      unsigned ra = ro + da, ry = ro + ya;
      dn[i] = (lds_rf4(ra) + lds_rf4(ra + 6144)) + (lds_rf4(ra + 12288) + lds_rf4(ra + 18432));
      y[i] = (lds_rf4(ry) + lds_rf4(ry + 8192)) + (lds_rf4(ry + 16384) + lds_rf4(ry + 24576));
    }
    TS12

    float dv[4] = {bflo(dk[0]), bfhi(dk[0]), bflo(dk[1]), bfhi(dk[1])};
#pragma unroll
    for (int i = 0; i < 2; i++) {
      float m = __uint_as_float(stg[i][2]), r = __uint_as_float(stg[i][3]);
      float xv[4] = {bflo(xe[i][0]), bfhi(xe[i][0]), bflo(xe[i][1]), bfhi(xe[i][1])};
      f32x4 o;
#pragma unroll
      for (int e = 0; e < 4; e++) {
        float xn = (xv[e] - m) * r;
        float s = __builtin_fmaf(xn, dv[e], y[i][e]);
        float g = dn[i][e] * __builtin_amdgcn_rcpf(1.f + __builtin_amdgcn_exp2f(-1.4426950408889634f * dn[i][e]));
        o[e] = __builtin_fmaf(s, g, xv[e]);
        if (i == 0 && e == 1) { TIE(o[e]);  } if (i == 0 && e == 3) { TIE(o[e]);  } if (i == 1 && e == 1) { TIE(o[e]);  }
      }
      GSTOUT(tko[i] * 2048 + (unsigned)(dq * 16), o, out + c.nt * 256);
    }
  }
}

extern "C" void kernel_launch(void* const* d_in, const int* in_sizes, int n_in, void* d_out, int out_size, void* d_ws,
                              size_t ws_size, hipStream_t stream) {
  s5_glu_fused<<<256, 512, 0, stream>>>((const char*)d_in[0], (const char*)d_in[1], (const char*)d_in[2],
                                        (const char*)d_in[3], (const char*)d_in[4], (const char*)d_in[5],
                                        (const char*)d_in[6], (const char*)d_in[7], (const char*)d_in[8],
                                        (const char*)d_in[10], (const char*)d_in[11], (const char*)d_in[12],
                                        (char*)d_out, (char*)d_ws);
}
```
